```python
import torch

def _softmax(x, axis=-1):
    return torch.softmax(x, dim=axis)
def _gelu(x):
    return torch.nn.functional.gelu(x, approximate="tanh")
def _silu(x):
    return torch.nn.functional.silu(x)
def _sigmoid(x):
    return torch.sigmoid(x)
def _layernorm(x, axis=-1, eps=1e-5):
    m = x.mean(dim=axis, keepdim=True)
    v = x.var(dim=axis, keepdim=True, unbiased=False)
    return (x - m) / torch.sqrt(v + eps)
def _scatter_add(tgt, idx, src, axis=0):
    return tgt.clone().index_add_(axis, idx, src)
def _segment_sum(src, seg, n):
    out = torch.zeros((n,) + src.shape[1:], dtype=src.dtype)
    return out.index_add_(0, seg, src)
def _pad(x, pw):
    flat = tuple(v for pair in reversed(pw) for v in pair)
    return torch.nn.functional.pad(x, flat)

kernel_name = 'gated_mix__ssm_conv_s4d__swiglu_ffn__prompt'

def setup_inputs():

    _w = (lambda a: (lambda t: t.to(torch.bfloat16) if t.is_floating_point() else t)(torch.as_tensor(a)))
    def _A_setup():
        Bsz, L, D, N = 1, 2048, 640, 192
        return {
            "u": _w((lambda *s: torch.randn(*s, dtype=torch.bfloat16) * 0.05)(Bsz, L, D)),
            "log_A_real": _w((lambda *s: torch.randn(*s, dtype=torch.bfloat16) * 6.4)(D, N) * 0.5),
            "A_imag": _w((lambda *s: torch.randn(*s, dtype=torch.bfloat16) * 0.05)(D, N) * 3.14),
            "C_real": _w((lambda *s: torch.randn(*s, dtype=torch.bfloat16) * 0.05)(D, N) / N ** 0.5),
            "C_imag": _w((lambda *s: torch.randn(*s, dtype=torch.bfloat16) * 0.05)(D, N) / N ** 0.5),
            "log_dt": _w((lambda *s: torch.randn(*s, dtype=torch.bfloat16) * 0.05)(D) * 0.5 - 4.0),
            "D_skip": _w((lambda *s: torch.randn(*s, dtype=torch.bfloat16) * 0.05)(D)),
        }
    _A_in = _A_setup()

    def _B_setup():
        B, S, D, F = 1, 2048, 640, 1536
        return {
            "x": _w((lambda *s: torch.randn(*s, dtype=torch.bfloat16) * 0.05)(B, S, D)),
            "w_gate": _w((lambda *s: torch.randn(*s, dtype=torch.bfloat16) * 0.05)(D, F)),
            "w_up": _w((lambda *s: torch.randn(*s, dtype=torch.bfloat16) * 0.05)(D, F)),
            "w_down": _w((lambda *s: torch.randn(*s, dtype=torch.bfloat16) * 0.05)(F, D)),
        }
    _B_in = _B_setup()

    return {
        "A_u": _A_in["u"],
        "A_log_A_real": _A_in["log_A_real"],
        "A_A_imag": _A_in["A_imag"],
        "A_C_real": _A_in["C_real"],
        "A_C_imag": _A_in["C_imag"],
        "A_log_dt": _A_in["log_dt"],
        "A_D_skip": _A_in["D_skip"],
        "B_x": _B_in["x"],
        "B_w_gate": _B_in["w_gate"],
        "B_w_up": _B_in["w_up"],
        "B_w_down": _B_in["w_down"],
        "gate_w": _w((lambda *s: torch.randn(*s, dtype=torch.bfloat16) * 0.05)(640) * 16.0),
    }

def reference(A_u, A_log_A_real, A_A_imag, A_C_real, A_C_imag, A_log_dt, A_D_skip, B_x, B_w_gate, B_w_up, B_w_down, gate_w):
    def _A_ref(u, log_A_real, A_imag, C_real, C_imag, log_dt, D_skip):
        L = u.shape[1]
        dt = torch.exp(log_dt)
        A = -torch.exp(log_A_real) + 1j * A_imag
        C = (C_real + 1j * C_imag) * dt[:, None]
        pos = (lambda x, dt: x.to(dt))(torch.arange(L), log_dt.dtype)
        P = torch.exp((A * dt[:, None])[:, :, None] * pos[None, None, :])
        K = 2.0 * torch.einsum("dn,dnl->dl", C, P).real
        L2 = 2 * L
        u_t = (lambda x, *d: x.permute(*d))(u, 0, 2, 1)
        Kf = (lambda x, *a, **k: torch.fft.rfft(x.float(), *a, **k))(K, L2)
        Uf = (lambda x, *a, **k: torch.fft.rfft(x.float(), *a, **k))(u_t, L2)
        y = (lambda x, *a, **k: torch.fft.irfft(x, *a, **k).to(torch.bfloat16))(Uf * Kf[None, :, :], L2)[:, :, :L]
        y = (lambda x, *d: x.permute(*d))(y, 0, 2, 1)
        return y + u * D_skip[None, None, :]

    def _B_ref(x, w_gate, w_up, w_down):
        g = _silu(x @ w_gate)
        u = x @ w_up
        return (g * u) @ w_down

    x = A_u
    g = _sigmoid((x * gate_w).sum(-1, keepdims=True))
    a = _A_ref(x, A_log_A_real, A_A_imag, A_C_real, A_C_imag, A_log_dt, A_D_skip)
    b = _B_ref(x, B_w_gate, B_w_up, B_w_down)
    return g * a + (1 - g) * b
```

```cpp
#include <hip/hip_runtime.h>
#include <hip/hip_bf16.h>
typedef unsigned short u16;
constexpr int L = 2048, D = 640, N = 192, F = 1536;
__device__ __forceinline__ float bf2f(u16 v) { return __uint_as_float(((unsigned)v) << 16); }
__device__ __forceinline__ u16 f2bf(float f) { unsigned u = __float_as_uint(f); u += 0x7FFFu + ((u >> 16) & 1u); return (u16)(u >> 16); }
__device__ __forceinline__ float rbf(float f) { return bf2f(f2bf(f)); }

__global__ void k_kgen(const u16* logA, const u16* Aim, const u16* Cre, const u16* Cim, const u16* logdt, float* K) {
    int d = blockIdx.x;
    __shared__ float sar[N], sai[N], scr[N], sci[N];
    float dt = rbf(expf(bf2f(logdt[d])));
    for (int n = threadIdx.x; n < N; n += blockDim.x) {
        float ar = -rbf(expf(bf2f(logA[d * N + n])));
        sar[n] = ar * dt; sai[n] = bf2f(Aim[d * N + n]) * dt;
        scr[n] = bf2f(Cre[d * N + n]) * dt; sci[n] = bf2f(Cim[d * N + n]) * dt;
    }
    __syncthreads();
    for (int l = threadIdx.x; l < L; l += blockDim.x) {
        float p = rbf((float)l), s = 0.f;
        for (int n = 0; n < N; ++n) {
            float e = expf(sar[n] * p), sn, cs; sincosf(sai[n] * p, &sn, &cs);
            s += scr[n] * (e * cs) - sci[n] * (e * sn);
        }
        K[d * L + l] = 2.f * s;
    }
}
__global__ void k_conv(const u16* u, const float* K, const u16* Dskip, u16* a) {
    int d = blockIdx.x * 64 + (threadIdx.x & 63);
    int l = blockIdx.y * 4 + (threadIdx.x >> 6);
    float s = 0.f;
    for (int j = 0; j <= l; ++j) s += K[d * L + j] * bf2f(u[(l - j) * D + d]);
    float y = rbf(s);
    a[l * D + d] = f2bf(y + rbf(bf2f(u[l * D + d]) * bf2f(Dskip[d])));
}
__global__ void k_g1(const u16* x, const u16* wg, const u16* wu, u16* h) {
    int f = blockIdx.x * 256 + threadIdx.x, l = blockIdx.y;
    float g = 0.f, up = 0.f;
    for (int k = 0; k < D; ++k) { float xv = bf2f(x[l * D + k]); g += xv * bf2f(wg[k * F + f]); up += xv * bf2f(wu[k * F + f]); }
    g = rbf(g); up = rbf(up);
    float s = rbf(g / (1.f + expf(-g)));
    h[l * F + f] = f2bf(s * up);
}
__global__ void k_g2(const u16* x, const u16* gw, const u16* h, const u16* wd, const u16* a, u16* out) {
    int l = blockIdx.x;
    __shared__ float red[128];
    float s = 0.f;
    for (int k = threadIdx.x; k < D; k += 128) s += rbf(bf2f(x[l * D + k]) * bf2f(gw[k]));
    red[threadIdx.x] = s; __syncthreads();
    for (int o = 64; o; o >>= 1) { if (threadIdx.x < o) red[threadIdx.x] += red[threadIdx.x + o]; __syncthreads(); }
    float z = rbf(red[0]);
    float g = rbf(1.f / (1.f + expf(-z)));
    float omg = rbf(1.f - g);
    for (int d = threadIdx.x; d < D; d += 128) {
        float b = 0.f;
        for (int k = 0; k < F; ++k) b += bf2f(h[l * F + k]) * bf2f(wd[k * D + d]);
        b = rbf(b);
        out[l * D + d] = f2bf(rbf(g * bf2f(a[l * D + d])) + rbf(omg * b));
    }
}
extern "C" void kernel_launch(void* const* d_in, const int* in_sizes, int n_in, void* d_out, int out_size, void* d_ws, size_t ws_size, hipStream_t stream) {
    const u16* u = (const u16*)d_in[0];
    float* K = (float*)d_ws;
    u16* a = (u16*)((char*)d_ws + (size_t)D * L * 4);
    u16* h = a + (size_t)L * D;
    k_kgen<<<D, 256, 0, stream>>>((const u16*)d_in[1], (const u16*)d_in[2], (const u16*)d_in[3], (const u16*)d_in[4], (const u16*)d_in[5], K);
    k_conv<<<dim3(D / 64, L / 4), 256, 0, stream>>>(u, K, (const u16*)d_in[6], a);
    k_g1<<<dim3(F / 256, L), 256, 0, stream>>>(u, (const u16*)d_in[8], (const u16*)d_in[9], h);
    k_g2<<<L, 128, 0, stream>>>(u, (const u16*)d_in[11], h, (const u16*)d_in[10], a, (u16*)d_out);
}
```

```cpp
#include <hip/hip_runtime.h>
typedef unsigned short u16;
typedef short bf16x8 __attribute__((ext_vector_type(8)));
typedef _Float16 f16x8 __attribute__((ext_vector_type(8)));
typedef _Float16 f16x2 __attribute__((ext_vector_type(2)));
typedef short s16x4 __attribute__((ext_vector_type(4)));
typedef float f32x4 __attribute__((ext_vector_type(4)));
typedef unsigned u32x4 __attribute__((ext_vector_type(4)));
typedef unsigned u32x2 __attribute__((ext_vector_type(2)));
#define LAS __attribute__((address_space(3)))
constexpr int L = 2048, D = 640, NM = 192, F = 1536;
constexpr size_t WS_H = 0, WS_WDF = 8u << 20, WS_UR = 12u << 20, WS_AT = 20u << 20, WS_G = 24u << 20, WS_STAMP = 128u << 20;
constexpr int URS = 2064;

#define WAIT_V(n) asm volatile("s_waitcnt vmcnt(" #n ")" ::: "memory")
#define WAIT_L(n) asm volatile("s_waitcnt lgkmcnt(" #n ")" ::: "memory")
#define BAR() do { asm volatile("" ::: "memory"); __builtin_amdgcn_s_barrier(); asm volatile("" ::: "memory"); } while (0)

__device__ __forceinline__ float bf2f(u16 v) { return __uint_as_float(((unsigned)v) << 16); }
__device__ __forceinline__ unsigned cvt_pk_bf16(float lo, float hi) { unsigned r; asm("v_cvt_pk_bf16_f32 %0, %1, %2" : "=v"(r) : "v"(lo), "v"(hi)); return r; }
__device__ __forceinline__ float rbf(float f) { return __uint_as_float(cvt_pk_bf16(f, f) << 16); }
__device__ __forceinline__ float lo16(unsigned v) { return __uint_as_float(v << 16); }
__device__ __forceinline__ float hi16(unsigned v) { return __uint_as_float(v & 0xffff0000u); }
__device__ __forceinline__ s16x4 tr_read(LAS const char* p) {
    typedef short v4i16_t __attribute__((ext_vector_type(4)));
    return __builtin_bit_cast(s16x4, __builtin_amdgcn_ds_read_tr16_b64_v4i16((LAS v4i16_t*)p));
}
__device__ __forceinline__ void stamp(unsigned long long* st, int i) {
}
#define RSRC(p, n) __builtin_amdgcn_make_buffer_rsrc((void*)(p), 0, (n), 0x00020000)

__global__ __launch_bounds__(256) void k_prep(const u16* __restrict__ u, const u16* __restrict__ wd, const u16* __restrict__ gw, char* __restrict__ ws) {
    const int b = blockIdx.x, tid = threadIdx.x;
    __shared__ u16 tile[64][66];
    if (b < 320) {
        const int l0 = (b / 10) * 64, d0 = (b % 10) * 64;
        for (int i = tid; i < 64 * 64; i += 256) tile[i >> 6][i & 63] = u[(l0 + (i >> 6)) * D + d0 + (i & 63)];
        __syncthreads();
        u16* ur = (u16*)(ws + WS_UR);
        for (int i = tid; i < 64 * 64; i += 256) {
            const int dd = i >> 6, tt = i & 63, l = l0 + 63 - tt, t = 2047 - l;
            const u16 v = tile[63 - tt][dd];
            ur[(size_t)(d0 + dd) * URS + t] = v;
            if (t >= 1) ur[(size_t)(D + d0 + dd) * URS + t - 1] = v;
        }
        if (l0 == 0) for (int i = tid; i < 64 * 16; i += 256) { const int dd = i >> 4, t = 2048 + (i & 15); ur[(size_t)(d0 + dd) * URS + t] = 0; ur[(size_t)(D + d0 + dd) * URS + t - 1] = 0; if ((i & 15) == 15) ur[(size_t)(D + d0 + dd) * URS + t] = 0; }
    } else if (b < 320 + 480) {
        const int fr = (b - 320) * 4 + (tid >> 6), lane = tid & 63, dt = fr / 48, S = fr % 48;
        const int tc = dt / 5, i5 = dt % 5, i = lane & 15, g = lane >> 4;
        const int d = tc * 80 + (i5 < 4 ? 32 * (i5 >> 1) + 8 * (i >> 2) + 4 * (i5 & 1) + (i & 3) : 64 + i);
        u32x4 o;
#pragma unroll
        for (int j = 0; j < 4; ++j) {
            const int f0 = 32 * S + 16 * (j >> 1) + 4 * g + 2 * (j & 1);
            o[j] = wd[f0 * D + d] | ((unsigned)wd[(f0 + 1) * D + d] << 16);
        }
        *(u32x4*)(ws + WS_WDF + (size_t)fr * 1024 + lane * 16) = o;
    } else {
        const int row = (b - 800) * 4 + (tid >> 6), lane = tid & 63;
        float s = 0.f;
        for (int k = lane; k < D; k += 64) s += rbf(bf2f(u[row * D + k]) * bf2f(gw[k]));
        for (int o = 32; o; o >>= 1) s += __shfl_xor(s, o);
        const float z = rbf(s);
        if (lane == 0) ((float*)(ws + WS_G))[row] = rbf(1.f / (1.f + __expf(-z)));
    }
}

constexpr int STAGE = 40960, XOFF = 0, GOFF = 16384, UOFF = 28672, NSLOT = 3, NKT = 10;
__global__ __launch_bounds__(512, 2) void k_g1(const u16* __restrict__ x, const u16* __restrict__ wg, const u16* __restrict__ wu, char* __restrict__ ws) {
    extern __shared__ __attribute__((aligned(16))) char smem[];
    LAS char* lds = (LAS char*)smem;
    const int tid = threadIdx.x, wid = __builtin_amdgcn_readfirstlane(tid >> 6), lane = tid & 63;
    const int b = blockIdx.x, xcd = b & 7, j = b >> 3;
    const int tm = 8 * (xcd & 1) + (j & 7), tn = 4 * (xcd >> 1) + (j >> 3);
    const int row0 = tm * 128, f0 = tn * 96;
    unsigned long long* st = (unsigned long long*)(ws + WS_STAMP) + (size_t)(256 * 8 + b * 8 + wid) * 16;
    stamp(st, 0);
    if (wid >= 4) {
        const int w = wid - 4;
        auto rx = RSRC(x, L * D * 2); auto rg = RSRC(wg, D * F * 2); auto ru = RSRC(wu, D * F * 2);
        const int vx = (row0 + 32 * w + (lane >> 3)) * 1280 + (((lane & 7) ^ (lane >> 3)) << 4);
        int vw[3];
#pragma unroll
        for (int i = 0; i < 3; ++i) { int P = 64 * (3 * w + i) + lane, k = P / 12, cp = P % 12, c = cp ^ ((k & 8) >> 2); vw[i] = k * 3072 + f0 * 2 + c * 16; }
        auto issue = [&](int t) {
            LAS char* sb = lds + (t % NSLOT) * STAGE;
#pragma unroll
            for (int i = 0; i < 4; ++i)
                __builtin_amdgcn_raw_ptr_buffer_load_lds(rx, sb + XOFF + (4 * w + i) * 1024, 16, vx, t * 128 + i * 10240, 0, 0);
#pragma unroll
            for (int i = 0; i < 3; ++i) {
                __builtin_amdgcn_raw_ptr_buffer_load_lds(rg, sb + GOFF + (3 * w + i) * 1024, 16, vw[i], t * 64 * 3072, 0, 0);
                __builtin_amdgcn_raw_ptr_buffer_load_lds(ru, sb + UOFF + (3 * w + i) * 1024, 16, vw[i], t * 64 * 3072, 0, 0);
            }
        };
        issue(0); issue(1); issue(2);
#pragma unroll
        for (int t = 0; t < NKT; ++t) {
            if (t == 0) WAIT_V(20); else if (t < NKT - 1) WAIT_V(10); else WAIT_V(0);
            BAR();
            if (t >= 1 && t + 2 < NKT) issue(t + 2);
        }
    } else {
        const int m = wid >> 1, n = wid & 1;
        const int kq = lane >> 4, q = (lane & 15) >> 2, p = lane & 3, bb = kq & 1;
        const int xa0 = XOFF + (64 * m + (lane & 15)) * 128 + ((kq ^ (lane & 7)) << 4);
        const int xa1 = xa0 ^ 64;
        const int wrow = (8 * kq + q) * 192 + (p & 1) * 8 + (p >> 1) * 16 + n * 96;
        const int waE = wrow + bb * 32, waO = wrow - bb * 32;
        f32x4 ag[4][3], au[4][3];
#pragma unroll
        for (int i = 0; i < 4; ++i)
#pragma unroll
            for (int t = 0; t < 3; ++t) { ag[i][t] = (f32x4){0, 0, 0, 0}; au[i][t] = (f32x4){0, 0, 0, 0}; }
        const int wa[3] = { (n & 1) ? waO : waE, (n & 1) ? waE : waO, (n & 1) ? waO : waE };
        for (int t = 0; t < NKT; ++t) {
            BAR();
            if (t == 0) stamp(st, 2);
            LAS const char* sb = lds + (t % NSLOT) * STAGE;
#pragma unroll
            for (int ks = 0; ks < 2; ++ks) {
                bf16x8 xb[4], wgf[3], wuf[3];
#pragma unroll
                for (int i = 0; i < 4; ++i) xb[i] = *(LAS const bf16x8*)(sb + (ks ? xa1 : xa0) + i * 2048);
#pragma unroll
                for (int tt = 0; tt < 3; ++tt) {
                    LAS const char* pg = sb + GOFF + wa[tt] + tt * 32 + ks * 6144;
                    s16x4 lo = tr_read(pg), hi = tr_read(pg + 768);
                    wgf[tt] = __builtin_shufflevector(lo, hi, 0, 1, 2, 3, 4, 5, 6, 7);
                    LAS const char* pu = sb + UOFF + wa[tt] + tt * 32 + ks * 6144;
                    lo = tr_read(pu); hi = tr_read(pu + 768);
                    wuf[tt] = __builtin_shufflevector(lo, hi, 0, 1, 2, 3, 4, 5, 6, 7);
                }
#pragma unroll
                for (int tt = 0; tt < 3; ++tt)
#pragma unroll
                    for (int i = 0; i < 4; ++i) {
                        ag[i][tt] = __builtin_amdgcn_mfma_f32_16x16x32_bf16(wgf[tt], xb[i], ag[i][tt], 0, 0, 0);
                        au[i][tt] = __builtin_amdgcn_mfma_f32_16x16x32_bf16(wuf[tt], xb[i], au[i][tt], 0, 0, 0);
                    }
            }
            WAIT_L(0);
        }
        stamp(st, 3);
        auto rh = RSRC(ws + WS_H, L * F * 2);
        const int tp0 = (row0 + 64 * m) / 32, ft0 = (f0 + 48 * n) / 16;
#pragma unroll
        for (int ip = 0; ip < 2; ++ip)
#pragma unroll
            for (int tt = 0; tt < 3; ++tt) {
                u32x4 o;
#pragma unroll
                for (int e = 0; e < 2; ++e) {
                    const f32x4 g = ag[2 * ip + e][tt], u = au[2 * ip + e][tt];
                    float h[4];
#pragma unroll
                    for (int r = 0; r < 4; ++r) {
                        const float ex = __builtin_amdgcn_exp2f(g[r] * -1.44269504089f);
                        h[r] = g[r] * u[r] * __builtin_amdgcn_rcpf(1.f + ex);
                    }
                    o[2 * e] = cvt_pk_bf16(h[0], h[1]); o[2 * e + 1] = cvt_pk_bf16(h[2], h[3]);
                }
                const int off = (((tp0 + ip) * 96 + ft0 + tt) * 64 + lane) * 16;
                __builtin_amdgcn_raw_buffer_store_b128(o, rh, off, 0, 0);
            }
        stamp(st, 4);
    }
}

struct cf { float r, i; };
__device__ __forceinline__ cf cmul(cf a, cf b) { return { __builtin_fmaf(a.r, b.r, -(a.i * b.i)), __builtin_fmaf(a.r, b.i, a.i * b.r) }; }
__device__ __forceinline__ cf zpow(float a2, float tr, float p) {
    const float e = __builtin_amdgcn_exp2f(a2 * p), r = __builtin_amdgcn_fractf(tr * p);
    return { e * __builtin_amdgcn_cosf(r), e * __builtin_amdgcn_sinf(r) };
}
__device__ __forceinline__ unsigned pk_f16(cf a) { f16x2 h = { (_Float16)a.r, (_Float16)a.i }; return __builtin_bit_cast(unsigned, h); }

constexpr int URC = 4160;
constexpr int S_UR = 0, S_KX = 4 * URC, S_Y = S_KX + 4608, S_X = S_Y + 4608, S_WAVE = S_X + 4608;
constexpr int PRS = 288;

__device__ __forceinline__ void ssm_channel(LAS char* wl, int d, int lane, const u16* __restrict__ logA, const u16* __restrict__ Aim, const u16* __restrict__ Cre,
                                            const u16* __restrict__ Cim, const u16* __restrict__ logdt, const u16* __restrict__ Dskip, char* __restrict__ ws, unsigned long long* st) {
    {
        auto rur = RSRC(ws + WS_UR, 2 * D * URS * 2);
#pragma unroll
        for (int c = 0; c < 4; ++c)
#pragma unroll
            for (int i = 0; i < 4; ++i)
                __builtin_amdgcn_raw_ptr_buffer_load_lds(rur, wl + S_UR + c * URC + i * 1024, 16, lane * 16, (((c & 1) * D + d) * URS + (c & 2)) * 2 + i * 1024, 0, 0);
    }
    const float dt = rbf(expf(bf2f(logdt[d])));
    float a2[3], tr[3]; cf C[3]; float mx = 0.f;
#pragma unroll
    for (int i = 0; i < 3; ++i) {
        const int o = d * NM + 64 * i + lane;
        const float ar = -rbf(expf(bf2f(logA[o])));
        a2[i] = (ar * dt) * 1.44269504089f; tr[i] = (bf2f(Aim[o]) * dt) * 0.15915494309f;
        C[i].r = bf2f(Cre[o]) * dt; C[i].i = bf2f(Cim[o]) * dt;
        mx = fmaxf(mx, fmaxf(fabsf(C[i].r), fabsf(C[i].i)));
    }
    const float dsk = bf2f(Dskip[d]);
#pragma unroll
    for (int o = 32; o; o >>= 1) mx = fmaxf(mx, __shfl_xor(mx, o));
    int ex = (int)(__float_as_uint(mx) >> 23); ex = ex < 13 ? 13 : ex;
    const float scale = __uint_as_float((unsigned)(267 - ex) << 23), unscale = __uint_as_float((unsigned)(ex - 12) << 23);
    stamp(st, 1);
    f32x4 acc[3] = { {0, 0, 0, 0}, {0, 0, 0, 0}, {0, 0, 0, 0} };
    LAS char* wy = wl + S_Y + lane * 4; LAS char* wx = wl + S_X + lane * 4;
    const int rd = (lane & 15) * PRS + (lane >> 4) * 16;
#define PUT(base, row, v) (*(LAS unsigned*)((base) + (row) * PRS) = pk_f16(v))
#pragma unroll
    for (int i = 0; i < 3; ++i) {
        const cf z1 = zpow(a2[i], tr[i], 1.f), z8 = zpow(a2[i], tr[i], 8.f), z128 = zpow(a2[i], tr[i], 128.f), z1024 = zpow(a2[i], tr[i], 1024.f);
        const cf z2 = cmul(z1, z1), z4 = cmul(z2, z2), z256 = cmul(z128, z128), z512 = cmul(z256, z256);
        {
            const cf w8 = { z8.r, -z8.i }, w16 = cmul(w8, w8);
            cf e = { 1.f, 0.f }, o = w8;
            PUT(wy, 0, e); PUT(wy, 1, o);
            e = w16; o = cmul(o, w16); PUT(wy, 2, e); PUT(wy, 3, o);
#pragma unroll
            for (int bq = 2; bq < 8; ++bq) { e = cmul(e, w16); o = cmul(o, w16); PUT(wy, 2 * bq, e); PUT(wy, 2 * bq + 1, o); }
        }
        f16x8 yb[4];
#pragma unroll
        for (int ks = 0; ks < 4; ++ks) yb[ks] = *(LAS const f16x8*)(wl + S_Y + rd + ks * 64);
        const cf C0 = { C[i].r * scale, C[i].i * scale };
        {
            cf v = C0;
#pragma unroll
            for (int c = 0; c < 8; ++c) { PUT(wx, c, v); const cf v2 = cmul(v, z128); PUT(wx, 8 + c, v2); if (c < 7) v = cmul(v, z1); }
#pragma unroll
            for (int ks = 0; ks < 4; ++ks) acc[0] = __builtin_amdgcn_mfma_f32_16x16x32_f16(*(LAS const f16x8*)(wl + S_X + rd + ks * 64), yb[ks], acc[0], 0, 0, 0);
        }
        {
            cf v = cmul(C0, z256);
#pragma unroll
            for (int c = 0; c < 4; ++c) { PUT(wx, c, v); const cf v2 = cmul(v, z128); PUT(wx, 4 + c, v2); if (c < 3) v = cmul(v, z2); }
            cf p0 = cmul(C0, z512), p1 = cmul(p0, z4);
#pragma unroll
            for (int a = 0; a < 4; ++a) { PUT(wx, 8 + 2 * a, p0); PUT(wx, 9 + 2 * a, p1); if (a < 3) { p0 = cmul(p0, z128); p1 = cmul(p1, z128); } }
#pragma unroll
            for (int ks = 0; ks < 4; ++ks) acc[1] = __builtin_amdgcn_mfma_f32_16x16x32_f16(*(LAS const f16x8*)(wl + S_X + rd + ks * 64), yb[ks], acc[1], 0, 0, 0);
        }
        {
            cf v = cmul(C0, z1024);
#pragma unroll
            for (int a = 0; a < 9; ++a) { PUT(wx, a, v); if (a < 8) v = cmul(v, z128); }
#pragma unroll
            for (int ks = 0; ks < 4; ++ks) acc[2] = __builtin_amdgcn_mfma_f32_16x16x32_f16(*(LAS const f16x8*)(wl + S_X + rd + ks * 64), yb[ks], acc[2], 0, 0, 0);
        }
    }
#undef PUT
    stamp(st, 2);
    {
        const int bq = lane & 15, g = lane >> 4;
        LAS char* kx = wl + S_KX; LAS char* kc = wl + S_Y;
        *(LAS u32x2*)(kx + lane * 8) = (u32x2){0, 0};
        {
            const unsigned w0 = cvt_pk_bf16(acc[1][0] * unscale, acc[1][1] * unscale), w1 = cvt_pk_bf16(acc[1][2] * unscale, acc[1][3] * unscale);
            const int q0 = g < 2 ? 64 * g + 4 * bq : 128 + 64 * (g - 2) + 2 * bq, q1 = g < 2 ? q0 + 2 : q0 + 32;
            *(LAS unsigned*)(kc + q0 * 2) = w0; *(LAS unsigned*)(kc + q1 * 2) = w1;
            const unsigned x0 = cvt_pk_bf16(acc[2][0] * unscale, acc[2][1] * unscale), x1 = cvt_pk_bf16(acc[2][2] * unscale, acc[2][3] * unscale);
            LAS u16* k2 = (LAS u16*)(kc + (256 + 64 * g + bq) * 2);
            k2[0] = (u16)x0; k2[16] = (u16)(x0 >> 16); k2[32] = (u16)x1; k2[48] = (u16)(x1 >> 16);
        }
        {
            const int p0 = (g < 2 ? 0 : 128) + 8 * bq + 4 * (g & 1);
            *(LAS u32x2*)(kx + (256 + p0) * 2) = (u32x2){ cvt_pk_bf16(acc[0][0] * unscale, acc[0][1] * unscale), cvt_pk_bf16(acc[0][2] * unscale, acc[0][3] * unscale) };
        }
        if (lane < 32) {
            const int i = 32 + lane, q0 = 4 * (i - 32);
            const u32x2 w = *(LAS const u32x2*)(kc + q0 * 2); const unsigned v4 = *(LAS const u16*)(kc + (q0 + 4) * 2);
            *(LAS u32x4*)(kx + (256 + 8 * i) * 2) = (u32x4){ __builtin_amdgcn_perm(w.x, w.x, 0x01000100), __builtin_amdgcn_perm(w.y, w.x, 0x05040302),
                                                           __builtin_amdgcn_perm(w.y, w.y, 0x01000100), __builtin_amdgcn_perm(v4, w.y, 0x05040302) };
        }
        {
            const int i = 64 + lane, q0 = 128 + 2 * lane;
            const unsigned w = *(LAS const unsigned*)(kc + q0 * 2), cc = *(LAS const u16*)(kc + (q0 + 2) * 2);
            *(LAS u32x4*)(kx + (256 + 8 * i) * 2) = (u32x4){ __builtin_amdgcn_perm(w, w, 0x01000100), w, __builtin_amdgcn_perm(w, w, 0x03020302), __builtin_amdgcn_perm(cc, cc, 0x01000100) };
        }
#pragma unroll
        for (int h = 0; h < 2; ++h) {
            const int i = 128 + 64 * h + lane, q0 = 256 + 64 * h + lane;
            const unsigned A = *(LAS const u16*)(kc + q0 * 2), B = *(LAS const u16*)(kc + (q0 + 1) * 2);
            const unsigned AA = A | (A << 16), BB = B | (B << 16);
            *(LAS u32x4*)(kx + (256 + 8 * i) * 2) = (u32x4){ AA, AA, (lane & 1) ? BB : (A | (B << 16)), BB };
        }
        if (lane < 8) *(LAS u32x4*)(wl + S_UR + (lane >> 1) * URC + 4096 + (lane & 1) * 16) = (u32x4){0, 0, 0, 0};
    }
    WAIT_V(0);
    stamp(st, 3);
    f32x4 y[8];
#pragma unroll
    for (int c = 0; c < 8; ++c) y[c] = (f32x4){0, 0, 0, 0};
    {
        const int r = lane & 15, kq = lane >> 4;
        LAS const char* ab = wl + S_UR + (3 - (r & 3)) * URC + (511 - 8 * 63 - 4 * (kq >> 1) + 2 * (kq & 1) - (r >> 2)) * 8;
        LAS const char* bbp = wl + S_KX + 512 + 32 * r - 32 * (kq >> 1) + 16 * (kq & 1) - 64 * 7;
#pragma unroll
        for (int rho = 0; rho < 8; ++rho) {
            bf16x8 A[8], B[8];
#pragma unroll
            for (int mu = 0; mu < 8; ++mu) {
                const int M = 8 * mu + rho;
                const s16x4 lo = *(LAS const s16x4*)(ab + (63 - M) * 64), hi = *(LAS const s16x4*)(ab + (63 - M) * 64 + 8);
                A[mu] = __builtin_shufflevector(lo, hi, 0, 1, 2, 3, 4, 5, 6, 7);
                B[mu] = *(LAS const bf16x8*)(bbp + (8 * mu - rho + 7) * 64);
            }
#pragma unroll
            for (int mu = 0; mu < 8; ++mu)
#pragma unroll
                for (int c = mu; c < 8; ++c) y[c] = __builtin_amdgcn_mfma_f32_16x16x32_bf16(A[mu], B[c - mu], y[c], 0, 0, 0);
        }
    }
    stamp(st, 4);
    {
        const int col = lane & 15, g = lane >> 4;
        auto rat = RSRC(ws + WS_AT, D * L * 2);
#pragma unroll
        for (int c = 0; c < 8; ++c) {
            const int l = 256 * c + 16 * col + 4 * g;
            const u32x2 uu = *(LAS const u32x2*)(wl + S_UR + (2044 - l) * 2);
            const unsigned y01 = cvt_pk_bf16(y[c][0], y[c][1]), y23 = cvt_pk_bf16(y[c][2], y[c][3]);
            const unsigned s01 = cvt_pk_bf16(hi16(uu.y) * dsk, lo16(uu.y) * dsk), s23 = cvt_pk_bf16(hi16(uu.x) * dsk, lo16(uu.x) * dsk);
            const u32x2 o = { cvt_pk_bf16(lo16(y01) + lo16(s01), hi16(y01) + hi16(s01)), cvt_pk_bf16(lo16(y23) + lo16(s23), hi16(y23) + hi16(s23)) };
            __builtin_amdgcn_raw_buffer_store_b64(o, rat, (d * L + l) * 2, 0, 0);
        }
    }
    stamp(st, 5);
}

__global__ __launch_bounds__(256, 1) void k_ssm(const u16* __restrict__ logA, const u16* __restrict__ Aim, const u16* __restrict__ Cre, const u16* __restrict__ Cim,
                                                const u16* __restrict__ logdt, const u16* __restrict__ Dskip, char* __restrict__ ws) {
    extern __shared__ __attribute__((aligned(16))) char smem[];
    const int wid = __builtin_amdgcn_readfirstlane(threadIdx.x >> 6), lane = threadIdx.x & 63;
    unsigned long long* st = (unsigned long long*)(ws + WS_STAMP) + (size_t)(512 * 8 + blockIdx.x * 8 + wid) * 16;
    stamp(st, 0);
    ssm_channel((LAS char*)smem + wid * S_WAVE, blockIdx.x * 4 + wid, lane, logA, Aim, Cre, Cim, logdt, Dskip, ws, st);
}

constexpr int G2_RED = 0, G2_AT = 4 * 3 * 5 * 1024, G2_LDS = G2_AT + 80 * 128;
__global__ __launch_bounds__(256, 1) void k_g2(char* __restrict__ ws, u16* __restrict__ out) {
    extern __shared__ __attribute__((aligned(16))) char smem[];
    LAS char* lds = (LAS char*)smem;
    const int tid = threadIdx.x, w = __builtin_amdgcn_readfirstlane(tid >> 6), lane = tid & 63;
    const int b = blockIdx.x, xcd = b & 7, j = b >> 3;
    const int tr = 8 * (xcd >> 1) + (j & 7), tc = 4 * (xcd & 1) + (j >> 3);
    unsigned long long* st = (unsigned long long*)(ws + WS_STAMP) + (size_t)(768 * 8 + b * 8 + w) * 16;
    stamp(st, 0);
    auto rh = RSRC(ws + WS_H, L * F * 2); auto rw = RSRC(ws + WS_WDF, F * D * 2); auto rat = RSRC(ws + WS_AT, D * L * 2);
    for (int i = w; i < 10; i += 4)
        __builtin_amdgcn_raw_ptr_buffer_load_lds(rat, lds + G2_AT + i * 1024, 16, ((tc * 80 + 8 * i + (lane >> 3)) * L + tr * 64) * 2 + (((lane & 7) ^ (lane >> 3)) << 4), 0, 0, 0);
    f32x4 acc[5][4];
#pragma unroll
    for (int i = 0; i < 5; ++i)
#pragma unroll
        for (int m = 0; m < 4; ++m) acc[i][m] = (f32x4){0, 0, 0, 0};
    const int vh = lane * 16;
    const int hb = (tr * 2 * 96) * 1024, wb = (tc * 5 * 48) * 1024;
    u32x4 hf[3][4], wf[3][5];
    auto load = [&](int slot, int S) {
        hf[slot][0] = __builtin_amdgcn_raw_buffer_load_b128(rh, vh, hb + (2 * S) * 1024, 0);
        hf[slot][1] = __builtin_amdgcn_raw_buffer_load_b128(rh, vh, hb + (2 * S + 1) * 1024, 0);
        hf[slot][2] = __builtin_amdgcn_raw_buffer_load_b128(rh, vh, hb + (96 + 2 * S) * 1024, 0);
        hf[slot][3] = __builtin_amdgcn_raw_buffer_load_b128(rh, vh, hb + (96 + 2 * S + 1) * 1024, 0);
#pragma unroll
        for (int i = 0; i < 5; ++i) wf[slot][i] = __builtin_amdgcn_raw_buffer_load_b128(rw, vh, wb + (i * 48 + S) * 1024, 0);
    };
    load(0, w); load(1, w + 4);
#pragma unroll
    for (int s = 0; s < 12; ++s) {
        if (s + 2 < 12) load((s + 2) % 3, w + 4 * (s + 2));
        const int sl = s % 3;
        bf16x8 hb4[4];
#pragma unroll
        for (int pr = 0; pr < 2; ++pr) {
            const u32x4 a = hf[sl][2 * pr], c = hf[sl][2 * pr + 1];
            hb4[2 * pr] = __builtin_bit_cast(bf16x8, (u32x4){ a.x, a.y, c.x, c.y });
            hb4[2 * pr + 1] = __builtin_bit_cast(bf16x8, (u32x4){ a.z, a.w, c.z, c.w });
        }
#pragma unroll
        for (int i = 0; i < 5; ++i)
#pragma unroll
            for (int m = 0; m < 4; ++m)
                acc[i][m] = __builtin_amdgcn_mfma_f32_16x16x32_bf16(__builtin_bit_cast(bf16x8, wf[sl][i]), hb4[m], acc[i][m], 0, 0, 0);
        if (s == 0) stamp(st, 1);
    }
    stamp(st, 2);
#pragma unroll
    for (int m = 0; m < 4; ++m)
        if (m != w) {
            const int slot = (w - m + 3) & 3;
#pragma unroll
            for (int i = 0; i < 5; ++i) *(LAS f32x4*)(lds + G2_RED + ((m * 3 + slot) * 5 + i) * 1024 + lane * 16) = acc[i][m];
        }
    WAIT_V(0); WAIT_L(0); BAR();
    f32x4 bsum[5];
#pragma unroll
    for (int i = 0; i < 5; ++i) {
        f32x4 s = w == 0 ? acc[i][0] : w == 1 ? acc[i][1] : w == 2 ? acc[i][2] : acc[i][3];
#pragma unroll
        for (int sl = 0; sl < 3; ++sl) s += *(LAS const f32x4*)(lds + G2_RED + ((w * 3 + sl) * 5 + i) * 1024 + lane * 16);
        bsum[i] = s;
    }
    stamp(st, 3);
    {
        const int tok = lane & 15, g = lane >> 4;
        const int l = tr * 64 + 16 * w + tok;
        const float gt = ((const float*)(ws + WS_G))[l], og = rbf(1.f - gt);
        const int q = tok >> 2, p = tok & 3;
        unsigned o[10];
#pragma unroll
        for (int i = 0; i < 5; ++i) {
            const int drow = (i < 4 ? 32 * (i >> 1) + 8 * g + 4 * (i & 1) : 64 + 4 * g) + q;
            const int ch = 2 * w + (p >> 1);
            const s16x4 av = tr_read(lds + G2_AT + drow * 128 + ((ch ^ (drow & 7)) << 4) + (p & 1) * 8);
            const u32x2 a2 = __builtin_bit_cast(u32x2, av);
            const unsigned b01 = cvt_pk_bf16(bsum[i][0], bsum[i][1]), b23 = cvt_pk_bf16(bsum[i][2], bsum[i][3]);
            const unsigned ga01 = cvt_pk_bf16(gt * lo16(a2.x), gt * hi16(a2.x)), ga23 = cvt_pk_bf16(gt * lo16(a2.y), gt * hi16(a2.y));
            const unsigned ob01 = cvt_pk_bf16(og * lo16(b01), og * hi16(b01)), ob23 = cvt_pk_bf16(og * lo16(b23), og * hi16(b23));
            o[2 * i] = cvt_pk_bf16(lo16(ga01) + lo16(ob01), hi16(ga01) + hi16(ob01));
            o[2 * i + 1] = cvt_pk_bf16(lo16(ga23) + lo16(ob23), hi16(ga23) + hi16(ob23));
        }
        auto ro = RSRC(out, L * D * 2);
        const int ob = (l * D + tc * 80) * 2;
        __builtin_amdgcn_raw_buffer_store_b128((u32x4){ o[0], o[1], o[2], o[3] }, ro, ob + 16 * g, 0, 0);
        __builtin_amdgcn_raw_buffer_store_b128((u32x4){ o[4], o[5], o[6], o[7] }, ro, ob + 64 + 16 * g, 0, 0);
        __builtin_amdgcn_raw_buffer_store_b64((u32x2){ o[8], o[9] }, ro, ob + 128 + 8 * g, 0, 0);
    }
    stamp(st, 4);
}

extern "C" void kernel_launch(void* const* d_in, const int* in_sizes, int n_in, void* d_out, int out_size, void* d_ws, size_t ws_size, hipStream_t stream) {
    const u16* u = (const u16*)d_in[0];
    char* ws = (char*)d_ws;
    (void)hipFuncSetAttribute((const void*)k_g1, hipFuncAttributeMaxDynamicSharedMemorySize, NSLOT * STAGE);
    (void)hipFuncSetAttribute((const void*)k_ssm, hipFuncAttributeMaxDynamicSharedMemorySize, 4 * S_WAVE);
    (void)hipFuncSetAttribute((const void*)k_g2, hipFuncAttributeMaxDynamicSharedMemorySize, G2_LDS);
    k_prep<<<800 + 512, 256, 0, stream>>>(u, (const u16*)d_in[10], (const u16*)d_in[11], ws);
    k_g1<<<256, 512, NSLOT * STAGE, stream>>>(u, (const u16*)d_in[8], (const u16*)d_in[9], ws);
    k_ssm<<<160, 256, 4 * S_WAVE, stream>>>((const u16*)d_in[1], (const u16*)d_in[2], (const u16*)d_in[3], (const u16*)d_in[4], (const u16*)d_in[5], (const u16*)d_in[6], ws);
    k_g2<<<256, 256, G2_LDS, stream>>>(ws, (u16*)d_out);
}
```

```cpp
#include <hip/hip_runtime.h>
typedef unsigned short u16;
typedef short bf16x8 __attribute__((ext_vector_type(8)));
typedef _Float16 f16x8 __attribute__((ext_vector_type(8)));
typedef _Float16 f16x2 __attribute__((ext_vector_type(2)));
typedef short s16x4 __attribute__((ext_vector_type(4)));
typedef float f32x4 __attribute__((ext_vector_type(4)));
typedef unsigned u32x4 __attribute__((ext_vector_type(4)));
typedef unsigned u32x2 __attribute__((ext_vector_type(2)));
#define LAS __attribute__((address_space(3)))
constexpr int L = 2048, D = 640, NM = 192, F = 1536;
constexpr size_t WS_H = 0, WS_WDF = 8u << 20, WS_UR = 12u << 20, WS_AT = 20u << 20, WS_G = 24u << 20, WS_STAMP = 128u << 20;
constexpr int URS = 2064;

#define WAIT_V(n) asm volatile("s_waitcnt vmcnt(" #n ")" ::: "memory")
#define WAIT_L(n) asm volatile("s_waitcnt lgkmcnt(" #n ")" ::: "memory")
#define BAR() do { asm volatile("" ::: "memory"); __builtin_amdgcn_s_barrier(); asm volatile("" ::: "memory"); } while (0)

__device__ __forceinline__ float bf2f(u16 v) { return __uint_as_float(((unsigned)v) << 16); }
__device__ __forceinline__ unsigned cvt_pk_bf16(float lo, float hi) { unsigned r; asm("v_cvt_pk_bf16_f32 %0, %1, %2" : "=v"(r) : "v"(lo), "v"(hi)); return r; }
__device__ __forceinline__ float rbf(float f) { return __uint_as_float(cvt_pk_bf16(f, f) << 16); }
__device__ __forceinline__ float lo16(unsigned v) { return __uint_as_float(v << 16); }
__device__ __forceinline__ float hi16(unsigned v) { return __uint_as_float(v & 0xffff0000u); }
__device__ __forceinline__ s16x4 tr_read(LAS const char* p) {
    typedef short v4i16_t __attribute__((ext_vector_type(4)));
    return __builtin_bit_cast(s16x4, __builtin_amdgcn_ds_read_tr16_b64_v4i16((LAS v4i16_t*)p));
}
__device__ __forceinline__ void stamp(unsigned long long* st, int i) {
}
#define RSRC(p, n) __builtin_amdgcn_make_buffer_rsrc((void*)(p), 0, (n), 0x00020000)

__global__ __launch_bounds__(256) void k_prep(const u16* __restrict__ u, const u16* __restrict__ wd, const u16* __restrict__ gw, char* __restrict__ ws) {
    const int b = blockIdx.x, tid = threadIdx.x;
    __shared__ u16 tile[64][66];
    if (b < 320) {
        const int l0 = (b / 10) * 64, d0 = (b % 10) * 64;
        for (int i = tid; i < 64 * 64; i += 256) tile[i >> 6][i & 63] = u[(l0 + (i >> 6)) * D + d0 + (i & 63)];
        __syncthreads();
        u16* ur = (u16*)(ws + WS_UR);
        for (int i = tid; i < 64 * 64; i += 256) {
            const int dd = i >> 6, tt = i & 63, l = l0 + 63 - tt, t = 2047 - l;
            const u16 v = tile[63 - tt][dd];
            ur[(size_t)(d0 + dd) * URS + t] = v;
            if (t >= 1) ur[(size_t)(D + d0 + dd) * URS + t - 1] = v;
        }
        if (l0 == 0) for (int i = tid; i < 64 * 16; i += 256) { const int dd = i >> 4, t = 2048 + (i & 15); ur[(size_t)(d0 + dd) * URS + t] = 0; ur[(size_t)(D + d0 + dd) * URS + t - 1] = 0; if ((i & 15) == 15) ur[(size_t)(D + d0 + dd) * URS + t] = 0; }
    } else if (b < 320 + 480) {
        const int fr = (b - 320) * 4 + (tid >> 6), lane = tid & 63, dt = fr / 48, S = fr % 48;
        const int tc = dt / 5, i5 = dt % 5, i = lane & 15, g = lane >> 4;
        const int d = tc * 80 + (i5 < 4 ? 32 * (i5 >> 1) + 8 * (i >> 2) + 4 * (i5 & 1) + (i & 3) : 64 + i);
        u32x4 o;
#pragma unroll
        for (int j = 0; j < 4; ++j) {
            const int f0 = 32 * S + 16 * (j >> 1) + 4 * g + 2 * (j & 1);
            o[j] = wd[f0 * D + d] | ((unsigned)wd[(f0 + 1) * D + d] << 16);
        }
        *(u32x4*)(ws + WS_WDF + (size_t)fr * 1024 + lane * 16) = o;
    } else {
        const int row = (b - 800) * 4 + (tid >> 6), lane = tid & 63;
        float s = 0.f;
        for (int k = lane; k < D; k += 64) s += rbf(bf2f(u[row * D + k]) * bf2f(gw[k]));
        for (int o = 32; o; o >>= 1) s += __shfl_xor(s, o);
        const float z = rbf(s);
        if (lane == 0) ((float*)(ws + WS_G))[row] = rbf(1.f / (1.f + __expf(-z)));
    }
}

constexpr int STAGE = 40960, XOFF = 0, GOFF = 16384, UOFF = 28672, NSLOT = 3, NKT = 10;
__global__ __launch_bounds__(512, 2) void k_g1(const u16* __restrict__ x, const u16* __restrict__ wg, const u16* __restrict__ wu, char* __restrict__ ws) {
    extern __shared__ __attribute__((aligned(16))) char smem[];
    LAS char* lds = (LAS char*)smem;
    const int tid = threadIdx.x, wid = __builtin_amdgcn_readfirstlane(tid >> 6), lane = tid & 63;
    const int b = blockIdx.x, xcd = b & 7, j = b >> 3;
    const int tm = 8 * (xcd & 1) + (j & 7), tn = 4 * (xcd >> 1) + (j >> 3);
    const int row0 = tm * 128, f0 = tn * 96;
    unsigned long long* st = (unsigned long long*)(ws + WS_STAMP) + (size_t)(256 * 8 + b * 8 + wid) * 16;
    stamp(st, 0);
    if (wid >= 4) {
        const int w = wid - 4;
        auto rx = RSRC(x, L * D * 2); auto rg = RSRC(wg, D * F * 2); auto ru = RSRC(wu, D * F * 2);
        const int vx = (row0 + 32 * w + (lane >> 3)) * 1280 + (((lane & 7) ^ (lane >> 3)) << 4);
        int vw[3];
#pragma unroll
        for (int i = 0; i < 3; ++i) { int P = 64 * (3 * w + i) + lane, k = P / 12, cp = P % 12, c = cp ^ ((k & 8) >> 2); vw[i] = k * 3072 + f0 * 2 + c * 16; }
        auto issue = [&](int t) {
            LAS char* sb = lds + (t % NSLOT) * STAGE;
#pragma unroll
            for (int i = 0; i < 4; ++i)
                __builtin_amdgcn_raw_ptr_buffer_load_lds(rx, sb + XOFF + (4 * w + i) * 1024, 16, vx, t * 128 + i * 10240, 0, 0);
#pragma unroll
            for (int i = 0; i < 3; ++i) {
                __builtin_amdgcn_raw_ptr_buffer_load_lds(rg, sb + GOFF + (3 * w + i) * 1024, 16, vw[i], t * 64 * 3072, 0, 0);
                __builtin_amdgcn_raw_ptr_buffer_load_lds(ru, sb + UOFF + (3 * w + i) * 1024, 16, vw[i], t * 64 * 3072, 0, 0);
            }
        };
        issue(0); issue(1); issue(2);
#pragma unroll
        for (int t = 0; t < NKT; ++t) {
            if (t == 0) WAIT_V(20); else if (t < NKT - 1) WAIT_V(10); else WAIT_V(0);
            BAR();
            if (t >= 1 && t + 2 < NKT) issue(t + 2);
        }
    } else {
        const int m = wid >> 1, n = wid & 1;
        const int kq = lane >> 4, q = (lane & 15) >> 2, p = lane & 3, bb = kq & 1;
        const int xa0 = XOFF + (64 * m + (lane & 15)) * 128 + ((kq ^ (lane & 7)) << 4);
        const int xa1 = xa0 ^ 64;
        const int wrow = (8 * kq + q) * 192 + (p & 1) * 8 + (p >> 1) * 16 + n * 96;
        const int waE = wrow + bb * 32, waO = wrow - bb * 32;
        f32x4 ag[4][3], au[4][3];
#pragma unroll
        for (int i = 0; i < 4; ++i)
#pragma unroll
            for (int t = 0; t < 3; ++t) { ag[i][t] = (f32x4){0, 0, 0, 0}; au[i][t] = (f32x4){0, 0, 0, 0}; }
        const int wa[3] = { (n & 1) ? waO : waE, (n & 1) ? waE : waO, (n & 1) ? waO : waE };
        for (int t = 0; t < NKT; ++t) {
            BAR();
            if (t == 0) stamp(st, 2);
            LAS const char* sb = lds + (t % NSLOT) * STAGE;
#pragma unroll
            for (int ks = 0; ks < 2; ++ks) {
                bf16x8 xb[4], wgf[3], wuf[3];
#pragma unroll
                for (int i = 0; i < 4; ++i) xb[i] = *(LAS const bf16x8*)(sb + (ks ? xa1 : xa0) + i * 2048);
#pragma unroll
                for (int tt = 0; tt < 3; ++tt) {
                    LAS const char* pg = sb + GOFF + wa[tt] + tt * 32 + ks * 6144;
                    s16x4 lo = tr_read(pg), hi = tr_read(pg + 768);
                    wgf[tt] = __builtin_shufflevector(lo, hi, 0, 1, 2, 3, 4, 5, 6, 7);
                    LAS const char* pu = sb + UOFF + wa[tt] + tt * 32 + ks * 6144;
                    lo = tr_read(pu); hi = tr_read(pu + 768);
                    wuf[tt] = __builtin_shufflevector(lo, hi, 0, 1, 2, 3, 4, 5, 6, 7);
                }
#pragma unroll
                for (int tt = 0; tt < 3; ++tt)
#pragma unroll
                    for (int i = 0; i < 4; ++i) {
                        ag[i][tt] = __builtin_amdgcn_mfma_f32_16x16x32_bf16(wgf[tt], xb[i], ag[i][tt], 0, 0, 0);
                        au[i][tt] = __builtin_amdgcn_mfma_f32_16x16x32_bf16(wuf[tt], xb[i], au[i][tt], 0, 0, 0);
                    }
            }
            WAIT_L(0);
        }
        stamp(st, 3);
        auto rh = RSRC(ws + WS_H, L * F * 2);
        const int tp0 = (row0 + 64 * m) / 32, ft0 = (f0 + 48 * n) / 16;
#pragma unroll
        for (int ip = 0; ip < 2; ++ip)
#pragma unroll
            for (int tt = 0; tt < 3; ++tt) {
                u32x4 o;
#pragma unroll
                for (int e = 0; e < 2; ++e) {
                    const f32x4 g = ag[2 * ip + e][tt], u = au[2 * ip + e][tt];
                    float h[4];
#pragma unroll
                    for (int r = 0; r < 4; ++r) {
                        const float ex = __builtin_amdgcn_exp2f(g[r] * -1.44269504089f);
                        h[r] = g[r] * u[r] * __builtin_amdgcn_rcpf(1.f + ex);
                    }
                    o[2 * e] = cvt_pk_bf16(h[0], h[1]); o[2 * e + 1] = cvt_pk_bf16(h[2], h[3]);
                }
                const int off = (((tp0 + ip) * 96 + ft0 + tt) * 64 + lane) * 16;
                __builtin_amdgcn_raw_buffer_store_b128(o, rh, off, 0, 0);
            }
        stamp(st, 4);
    }
}

struct cf { float r, i; };
__device__ __forceinline__ cf cmul(cf a, cf b) { return { __builtin_fmaf(a.r, b.r, -(a.i * b.i)), __builtin_fmaf(a.r, b.i, a.i * b.r) }; }
__device__ __forceinline__ cf zpow(float a2, float tr, float p) {
    const float e = __builtin_amdgcn_exp2f(a2 * p), r = __builtin_amdgcn_fractf(tr * p);
    return { e * __builtin_amdgcn_cosf(r), e * __builtin_amdgcn_sinf(r) };
}
__device__ __forceinline__ unsigned pk_f16(cf a) { f16x2 h = { (_Float16)a.r, (_Float16)a.i }; return __builtin_bit_cast(unsigned, h); }

constexpr int URC = 4160;
constexpr int S_UR = 0, S_KX = 4 * URC, S_Y = S_KX + 4608, S_X = S_Y + 4608, S_WAVE = S_X + 4608;
constexpr int PRS = 288;

__device__ __forceinline__ void ssm_channel(LAS char* wl, int d, int lane, const u16* __restrict__ logA, const u16* __restrict__ Aim, const u16* __restrict__ Cre,
                                            const u16* __restrict__ Cim, const u16* __restrict__ logdt, const u16* __restrict__ Dskip, char* __restrict__ ws, unsigned long long* st) {
    {
        auto rur = RSRC(ws + WS_UR, 2 * D * URS * 2);
#pragma unroll
        for (int c = 0; c < 4; ++c)
#pragma unroll
            for (int i = 0; i < 4; ++i)
                __builtin_amdgcn_raw_ptr_buffer_load_lds(rur, wl + S_UR + c * URC + i * 1024, 16, lane * 16, (((c & 1) * D + d) * URS + (c & 2)) * 2 + i * 1024, 0, 0);
    }
    const float dt = rbf(expf(bf2f(logdt[d])));
    float a2[3], tr[3]; cf C[3]; float mx = 0.f;
#pragma unroll
    for (int i = 0; i < 3; ++i) {
        const int o = d * NM + 64 * i + lane;
        const float ar = -rbf(expf(bf2f(logA[o])));
        a2[i] = (ar * dt) * 1.44269504089f; tr[i] = (bf2f(Aim[o]) * dt) * 0.15915494309f;
        C[i].r = bf2f(Cre[o]) * dt; C[i].i = bf2f(Cim[o]) * dt;
        mx = fmaxf(mx, fmaxf(fabsf(C[i].r), fabsf(C[i].i)));
    }
    const float dsk = bf2f(Dskip[d]);
#pragma unroll
    for (int o = 32; o; o >>= 1) mx = fmaxf(mx, __shfl_xor(mx, o));
    int ex = (int)(__float_as_uint(mx) >> 23); ex = ex < 13 ? 13 : ex;
    const float scale = __uint_as_float((unsigned)(267 - ex) << 23), unscale = __uint_as_float((unsigned)(ex - 12) << 23);
    stamp(st, 1);
    f32x4 acc[3] = { {0, 0, 0, 0}, {0, 0, 0, 0}, {0, 0, 0, 0} };
    LAS char* wy = wl + S_Y + lane * 4; LAS char* wx = wl + S_X + lane * 4;
    const int rd = (lane & 15) * PRS + (lane >> 4) * 16;
#define PUT(base, row, v) (*(LAS unsigned*)((base) + (row) * PRS) = pk_f16(v))
#pragma unroll
    for (int i = 0; i < 3; ++i) {
        const cf z1 = zpow(a2[i], tr[i], 1.f), z8 = zpow(a2[i], tr[i], 8.f), z128 = zpow(a2[i], tr[i], 128.f), z1024 = zpow(a2[i], tr[i], 1024.f);
        const cf z2 = cmul(z1, z1), z4 = cmul(z2, z2), z256 = cmul(z128, z128), z512 = cmul(z256, z256);
        {
            const cf w8 = { z8.r, -z8.i }, w16 = cmul(w8, w8);
            cf e = { 1.f, 0.f }, o = w8;
            PUT(wy, 0, e); PUT(wy, 1, o);
            e = w16; o = cmul(o, w16); PUT(wy, 2, e); PUT(wy, 3, o);
#pragma unroll
            for (int bq = 2; bq < 8; ++bq) { e = cmul(e, w16); o = cmul(o, w16); PUT(wy, 2 * bq, e); PUT(wy, 2 * bq + 1, o); }
        }
        f16x8 yb[4];
#pragma unroll
        for (int ks = 0; ks < 4; ++ks) yb[ks] = *(LAS const f16x8*)(wl + S_Y + rd + ks * 64);
        const cf C0 = { C[i].r * scale, C[i].i * scale };
        {
            cf v = C0;
#pragma unroll
            for (int c = 0; c < 8; ++c) { PUT(wx, c, v); const cf v2 = cmul(v, z128); PUT(wx, 8 + c, v2); if (c < 7) v = cmul(v, z1); }
#pragma unroll
            for (int ks = 0; ks < 4; ++ks) acc[0] = __builtin_amdgcn_mfma_f32_16x16x32_f16(*(LAS const f16x8*)(wl + S_X + rd + ks * 64), yb[ks], acc[0], 0, 0, 0);
        }
        {
            cf v = cmul(C0, z256);
#pragma unroll
            for (int c = 0; c < 4; ++c) { PUT(wx, c, v); const cf v2 = cmul(v, z128); PUT(wx, 4 + c, v2); if (c < 3) v = cmul(v, z2); }
            cf p0 = cmul(C0, z512), p1 = cmul(p0, z4);
#pragma unroll
            for (int a = 0; a < 4; ++a) { PUT(wx, 8 + 2 * a, p0); PUT(wx, 9 + 2 * a, p1); if (a < 3) { p0 = cmul(p0, z128); p1 = cmul(p1, z128); } }
#pragma unroll
            for (int ks = 0; ks < 4; ++ks) acc[1] = __builtin_amdgcn_mfma_f32_16x16x32_f16(*(LAS const f16x8*)(wl + S_X + rd + ks * 64), yb[ks], acc[1], 0, 0, 0);
        }
        {
            cf v = cmul(C0, z1024);
#pragma unroll
            for (int a = 0; a < 9; ++a) { PUT(wx, a, v); if (a < 8) v = cmul(v, z128); }
#pragma unroll
            for (int ks = 0; ks < 4; ++ks) acc[2] = __builtin_amdgcn_mfma_f32_16x16x32_f16(*(LAS const f16x8*)(wl + S_X + rd + ks * 64), yb[ks], acc[2], 0, 0, 0);
        }
    }
#undef PUT
    stamp(st, 2);
    {
        const int bq = lane & 15, g = lane >> 4;
        LAS char* kx = wl + S_KX; LAS char* kc = wl + S_Y;
        *(LAS u32x2*)(kx + lane * 8) = (u32x2){0, 0};
        {
            const unsigned w0 = cvt_pk_bf16(acc[1][0] * unscale, acc[1][1] * unscale), w1 = cvt_pk_bf16(acc[1][2] * unscale, acc[1][3] * unscale);
            const int q0 = g < 2 ? 64 * g + 4 * bq : 128 + 64 * (g - 2) + 2 * bq, q1 = g < 2 ? q0 + 2 : q0 + 32;
            *(LAS unsigned*)(kc + q0 * 2) = w0; *(LAS unsigned*)(kc + q1 * 2) = w1;
            const unsigned x0 = cvt_pk_bf16(acc[2][0] * unscale, acc[2][1] * unscale), x1 = cvt_pk_bf16(acc[2][2] * unscale, acc[2][3] * unscale);
            LAS u16* k2 = (LAS u16*)(kc + (256 + 64 * g + bq) * 2);
            k2[0] = (u16)x0; k2[16] = (u16)(x0 >> 16); k2[32] = (u16)x1; k2[48] = (u16)(x1 >> 16);
        }
        {
            const int p0 = (g < 2 ? 0 : 128) + 8 * bq + 4 * (g & 1);
            *(LAS u32x2*)(kx + (256 + p0) * 2) = (u32x2){ cvt_pk_bf16(acc[0][0] * unscale, acc[0][1] * unscale), cvt_pk_bf16(acc[0][2] * unscale, acc[0][3] * unscale) };
        }
        if (lane < 32) {
            const int i = 32 + lane, q0 = 4 * (i - 32);
            const u32x2 w = *(LAS const u32x2*)(kc + q0 * 2); const unsigned v4 = *(LAS const u16*)(kc + (q0 + 4) * 2);
            *(LAS u32x4*)(kx + (256 + 8 * i) * 2) = (u32x4){ __builtin_amdgcn_perm(w.x, w.x, 0x01000100), __builtin_amdgcn_perm(w.y, w.x, 0x05040302),
                                                           __builtin_amdgcn_perm(w.y, w.y, 0x01000100), __builtin_amdgcn_perm(v4, w.y, 0x05040302) };
        }
        {
            const int i = 64 + lane, q0 = 128 + 2 * lane;
            const unsigned w = *(LAS const unsigned*)(kc + q0 * 2), cc = *(LAS const u16*)(kc + (q0 + 2) * 2);
            *(LAS u32x4*)(kx + (256 + 8 * i) * 2) = (u32x4){ __builtin_amdgcn_perm(w, w, 0x01000100), w, __builtin_amdgcn_perm(w, w, 0x03020302), __builtin_amdgcn_perm(cc, cc, 0x01000100) };
        }
#pragma unroll
        for (int h = 0; h < 2; ++h) {
            const int i = 128 + 64 * h + lane, q0 = 256 + 64 * h + lane;
            const unsigned A = *(LAS const u16*)(kc + q0 * 2), B = *(LAS const u16*)(kc + (q0 + 1) * 2);
            const unsigned AA = A | (A << 16), BB = B | (B << 16);
            *(LAS u32x4*)(kx + (256 + 8 * i) * 2) = (u32x4){ AA, AA, (lane & 1) ? BB : (A | (B << 16)), BB };
        }
        if (lane < 8) *(LAS u32x4*)(wl + S_UR + (lane >> 1) * URC + 4096 + (lane & 1) * 16) = (u32x4){0, 0, 0, 0};
    }
    WAIT_V(0);
    stamp(st, 3);
    f32x4 y[8];
#pragma unroll
    for (int c = 0; c < 8; ++c) y[c] = (f32x4){0, 0, 0, 0};
    {
        const int r = lane & 15, kq = lane >> 4;
        LAS const char* ab = wl + S_UR + (3 - (r & 3)) * URC + (511 - 8 * 63 - 4 * (kq >> 1) + 2 * (kq & 1) - (r >> 2)) * 8;
        LAS const char* bbp = wl + S_KX + 512 + 32 * r - 32 * (kq >> 1) + 16 * (kq & 1) - 64 * 7;
#pragma unroll
        for (int rho = 0; rho < 8; ++rho) {
            bf16x8 A[8], B[8];
#pragma unroll
            for (int mu = 0; mu < 8; ++mu) {
                const int M = 8 * mu + rho;
                const s16x4 lo = *(LAS const s16x4*)(ab + (63 - M) * 64), hi = *(LAS const s16x4*)(ab + (63 - M) * 64 + 8);
                A[mu] = __builtin_shufflevector(lo, hi, 0, 1, 2, 3, 4, 5, 6, 7);
                B[mu] = *(LAS const bf16x8*)(bbp + (8 * mu - rho + 7) * 64);
            }
#pragma unroll
            for (int mu = 0; mu < 8; ++mu)
#pragma unroll
                for (int c = mu; c < 8; ++c) y[c] = __builtin_amdgcn_mfma_f32_16x16x32_bf16(A[mu], B[c - mu], y[c], 0, 0, 0);
        }
    }
    stamp(st, 4);
    {
        const int col = lane & 15, g = lane >> 4;
        auto rat = RSRC(ws + WS_AT, D * L * 2);
#pragma unroll
        for (int c = 0; c < 8; ++c) {
            const int l = 256 * c + 16 * col + 4 * g;
            const u32x2 uu = *(LAS const u32x2*)(wl + S_UR + (2044 - l) * 2);
            const unsigned y01 = cvt_pk_bf16(y[c][0], y[c][1]), y23 = cvt_pk_bf16(y[c][2], y[c][3]);
            const unsigned s01 = cvt_pk_bf16(hi16(uu.y) * dsk, lo16(uu.y) * dsk), s23 = cvt_pk_bf16(hi16(uu.x) * dsk, lo16(uu.x) * dsk);
            const u32x2 o = { cvt_pk_bf16(lo16(y01) + lo16(s01), hi16(y01) + hi16(s01)), cvt_pk_bf16(lo16(y23) + lo16(s23), hi16(y23) + hi16(s23)) };
            __builtin_amdgcn_raw_buffer_store_b64(o, rat, (d * L + l) * 2, 0, 0);
        }
    }
    stamp(st, 5);
}

__global__ __launch_bounds__(256, 1) void k_ssm(const u16* __restrict__ logA, const u16* __restrict__ Aim, const u16* __restrict__ Cre, const u16* __restrict__ Cim,
                                                const u16* __restrict__ logdt, const u16* __restrict__ Dskip, char* __restrict__ ws) {
    extern __shared__ __attribute__((aligned(16))) char smem[];
    const int wid = __builtin_amdgcn_readfirstlane(threadIdx.x >> 6), lane = threadIdx.x & 63;
    unsigned long long* st = (unsigned long long*)(ws + WS_STAMP) + (size_t)(512 * 8 + blockIdx.x * 8 + wid) * 16;
    stamp(st, 0);
    ssm_channel((LAS char*)smem + wid * S_WAVE, blockIdx.x * 4 + wid, lane, logA, Aim, Cre, Cim, logdt, Dskip, ws, st);
}

constexpr int G2_RED = 0, G2_AT = 4 * 3 * 5 * 1024, G2_LDS = G2_AT + 80 * 128;
__global__ __launch_bounds__(256, 1) void k_g2(char* __restrict__ ws, u16* __restrict__ out) {
    extern __shared__ __attribute__((aligned(16))) char smem[];
    LAS char* lds = (LAS char*)smem;
    const int tid = threadIdx.x, w = __builtin_amdgcn_readfirstlane(tid >> 6), lane = tid & 63;
    const int b = blockIdx.x, xcd = b & 7, j = b >> 3;
    const int tr = 8 * (xcd >> 1) + (j & 7), tc = 4 * (xcd & 1) + (j >> 3);
    unsigned long long* st = (unsigned long long*)(ws + WS_STAMP) + (size_t)(768 * 8 + b * 8 + w) * 16;
    stamp(st, 0);
    auto rh = RSRC(ws + WS_H, L * F * 2); auto rw = RSRC(ws + WS_WDF, F * D * 2); auto rat = RSRC(ws + WS_AT, D * L * 2);
    for (int i = w; i < 10; i += 4)
        __builtin_amdgcn_raw_ptr_buffer_load_lds(rat, lds + G2_AT + i * 1024, 16, ((tc * 80 + 8 * i + (lane >> 3)) * L + tr * 64) * 2 + (((lane & 7) ^ (lane >> 3)) << 4), 0, 0, 0);
    f32x4 acc[5][4];
#pragma unroll
    for (int i = 0; i < 5; ++i)
#pragma unroll
        for (int m = 0; m < 4; ++m) acc[i][m] = (f32x4){0, 0, 0, 0};
    const int vh = lane * 16;
    const int hb = (tr * 2 * 96) * 1024, wb = (tc * 5 * 48) * 1024;
    u32x4 hf[3][4], wf[3][5];
    auto load = [&](int slot, int S) {
        hf[slot][0] = __builtin_amdgcn_raw_buffer_load_b128(rh, vh, hb + (2 * S) * 1024, 0);
        hf[slot][1] = __builtin_amdgcn_raw_buffer_load_b128(rh, vh, hb + (2 * S + 1) * 1024, 0);
        hf[slot][2] = __builtin_amdgcn_raw_buffer_load_b128(rh, vh, hb + (96 + 2 * S) * 1024, 0);
        hf[slot][3] = __builtin_amdgcn_raw_buffer_load_b128(rh, vh, hb + (96 + 2 * S + 1) * 1024, 0);
#pragma unroll
        for (int i = 0; i < 5; ++i) wf[slot][i] = __builtin_amdgcn_raw_buffer_load_b128(rw, vh, wb + (i * 48 + S) * 1024, 0);
    };
    load(0, w); load(1, w + 4);
#pragma unroll
    for (int s = 0; s < 12; ++s) {
        if (s + 2 < 12) load((s + 2) % 3, w + 4 * (s + 2));
        const int sl = s % 3;
        bf16x8 hb4[4];
#pragma unroll
        for (int pr = 0; pr < 2; ++pr) {
            const u32x4 a = hf[sl][2 * pr], c = hf[sl][2 * pr + 1];
            hb4[2 * pr] = __builtin_bit_cast(bf16x8, (u32x4){ a.x, a.y, c.x, c.y });
            hb4[2 * pr + 1] = __builtin_bit_cast(bf16x8, (u32x4){ a.z, a.w, c.z, c.w });
        }
#pragma unroll
        for (int i = 0; i < 5; ++i)
#pragma unroll
            for (int m = 0; m < 4; ++m)
                acc[i][m] = __builtin_amdgcn_mfma_f32_16x16x32_bf16(__builtin_bit_cast(bf16x8, wf[sl][i]), hb4[m], acc[i][m], 0, 0, 0);
        if (s == 0) stamp(st, 1);
    }
    stamp(st, 2);
#pragma unroll
    for (int m = 0; m < 4; ++m)
        if (m != w) {
            const int slot = (w - m + 3) & 3;
#pragma unroll
            for (int i = 0; i < 5; ++i) *(LAS f32x4*)(lds + G2_RED + ((m * 3 + slot) * 5 + i) * 1024 + lane * 16) = acc[i][m];
        }
    WAIT_V(0); WAIT_L(0); BAR();
    f32x4 bsum[5];
#pragma unroll
    for (int i = 0; i < 5; ++i) {
        f32x4 s = w == 0 ? acc[i][0] : w == 1 ? acc[i][1] : w == 2 ? acc[i][2] : acc[i][3];
#pragma unroll
        for (int sl = 0; sl < 3; ++sl) s += *(LAS const f32x4*)(lds + G2_RED + ((w * 3 + sl) * 5 + i) * 1024 + lane * 16);
        bsum[i] = s;
    }
    stamp(st, 3);
    {
        const int tok = lane & 15, g = lane >> 4;
        const int l = tr * 64 + 16 * w + tok;
        const float gt = ((const float*)(ws + WS_G))[l], og = rbf(1.f - gt);
        const int q = tok >> 2, p = tok & 3;
        unsigned o[10];
#pragma unroll
        for (int i = 0; i < 5; ++i) {
            const int drow = (i < 4 ? 32 * (i >> 1) + 8 * g + 4 * (i & 1) : 64 + 4 * g) + q;
            const int ch = 2 * w + (p >> 1);
            const s16x4 av = tr_read(lds + G2_AT + drow * 128 + ((ch ^ (drow & 7)) << 4) + (p & 1) * 8);
            const u32x2 a2 = __builtin_bit_cast(u32x2, av);
            const unsigned b01 = cvt_pk_bf16(bsum[i][0], bsum[i][1]), b23 = cvt_pk_bf16(bsum[i][2], bsum[i][3]);
            const unsigned ga01 = cvt_pk_bf16(gt * lo16(a2.x), gt * hi16(a2.x)), ga23 = cvt_pk_bf16(gt * lo16(a2.y), gt * hi16(a2.y));
            const unsigned ob01 = cvt_pk_bf16(og * lo16(b01), og * hi16(b01)), ob23 = cvt_pk_bf16(og * lo16(b23), og * hi16(b23));
            o[2 * i] = cvt_pk_bf16(lo16(ga01) + lo16(ob01), hi16(ga01) + hi16(ob01));
            o[2 * i + 1] = cvt_pk_bf16(lo16(ga23) + lo16(ob23), hi16(ga23) + hi16(ob23));
        }
        auto ro = RSRC(out, L * D * 2);
        const int ob = (l * D + tc * 80) * 2;
        __builtin_amdgcn_raw_buffer_store_b128((u32x4){ o[0], o[1], o[2], o[3] }, ro, ob + 16 * g, 0, 0);
        __builtin_amdgcn_raw_buffer_store_b128((u32x4){ o[4], o[5], o[6], o[7] }, ro, ob + 64 + 16 * g, 0, 0);
        __builtin_amdgcn_raw_buffer_store_b64((u32x2){ o[8], o[9] }, ro, ob + 128 + 8 * g, 0, 0);
    }
    stamp(st, 4);
}

template <int NI>
__global__ __launch_bounds__(512) void k_probe(const char* __restrict__ src, int xcd_stride, int cu_stride, unsigned* sink) {
    extern __shared__ __attribute__((aligned(16))) char smem[];
    LAS char* lds = (LAS char*)smem;
    const int wid = __builtin_amdgcn_readfirstlane(threadIdx.x >> 6), lane = threadIdx.x & 63;
    const int xcd = blockIdx.x & 7, j = blockIdx.x >> 3;
    if (NI > 0) {
        auto r = __builtin_amdgcn_make_buffer_rsrc((void*)src, 0, 0x7fffffff, 0x00020000);
        const int base = xcd * xcd_stride + j * cu_stride + wid * NI * 1024;
#pragma unroll
        for (int i = 0; i < NI; ++i) __builtin_amdgcn_raw_ptr_buffer_load_lds(r, lds + (wid * NI + i) * 1024, 16, lane * 16, base + i * 1024, 0, 0);
        asm volatile("s_waitcnt vmcnt(0)" ::: "memory");
    }
}
extern "C" void kernel_launch(void* const* d_in, const int* in_sizes, int n_in, void* d_out, int out_size, void* d_ws, size_t ws_size, hipStream_t stream) {
    const u16* u = (const u16*)d_in[0];
    char* ws = (char*)d_ws;
    (void)hipFuncSetAttribute((const void*)k_g1, hipFuncAttributeMaxDynamicSharedMemorySize, NSLOT * STAGE);
    (void)hipFuncSetAttribute((const void*)k_ssm, hipFuncAttributeMaxDynamicSharedMemorySize, 4 * S_WAVE);
    (void)hipFuncSetAttribute((const void*)k_g2, hipFuncAttributeMaxDynamicSharedMemorySize, G2_LDS);
    (void)hipFuncSetAttribute((const void*)k_probe<1>, hipFuncAttributeMaxDynamicSharedMemorySize, 98304);
    k_probe<1><<<256, 512, 98304, stream>>>((const char*)d_in[7], 32 * 8192, 8192, nullptr);
    k_prep<<<800 + 512, 256, 0, stream>>>(u, (const u16*)d_in[10], (const u16*)d_in[11], ws);
    k_g1<<<256, 512, NSLOT * STAGE, stream>>>(u, (const u16*)d_in[8], (const u16*)d_in[9], ws);
    k_ssm<<<160, 256, 4 * S_WAVE, stream>>>((const u16*)d_in[1], (const u16*)d_in[2], (const u16*)d_in[3], (const u16*)d_in[4], (const u16*)d_in[5], (const u16*)d_in[6], ws);
    k_g2<<<256, 256, G2_LDS, stream>>>(ws, (u16*)d_out);
}
```

```cpp
#include <hip/hip_runtime.h>
typedef unsigned short u16;
typedef unsigned long long u64;
typedef short bf16x8 __attribute__((ext_vector_type(8)));
typedef _Float16 f16x8 __attribute__((ext_vector_type(8)));
typedef _Float16 f16x2 __attribute__((ext_vector_type(2)));
typedef short s16x4 __attribute__((ext_vector_type(4)));
typedef float f32x4 __attribute__((ext_vector_type(4)));
typedef float f32x2 __attribute__((ext_vector_type(2)));
typedef unsigned u32x4 __attribute__((ext_vector_type(4)));
typedef unsigned u32x2 __attribute__((ext_vector_type(2)));
#define LAS __attribute__((address_space(3)))
#define GAS __attribute__((address_space(1)))
constexpr int L = 2048, D = 640, NM = 192, F = 1536;

constexpr size_t WS_H = 0, WS_WDF = 8u << 20, WS_UR = 12u << 20, WS_AT = 20u << 20, WS_G = 24u << 20, WS_FLAG = 25u << 20, WS_EPOCH = WS_FLAG + 65536, WS_STAMP = 128u << 20;
constexpr int URS = 2112;
constexpr int FH = 0, FU = 1024, FW = 1536, FG = 1792, FA = 2048;
constexpr int SC1 = 16;

constexpr int STAGE = 40960, XOFF = 0, GOFF = 16384, UOFF = 28672, NSLOT = 3, NKT = 10;
constexpr int P_U = 122880, P_W = P_U + 11264;
constexpr int S_PART = 147456;
constexpr int URC = 4160, PRS = 288;
constexpr int S_SCR = 40960, SCR_W = 9216;
constexpr int S_AST = 61440;
constexpr int S_PER = 78848, PER_W = 4 * URC + 4608, S_KX = 4 * URC;
constexpr int S_SYNC = S_PER + 3 * PER_W + 4128;
constexpr int LDS_BYTES = 163840;

#define WAIT_V(n) asm volatile("s_waitcnt vmcnt(" #n ")" ::: "memory")
#define WAIT_L(n) asm volatile("s_waitcnt lgkmcnt(" #n ")" ::: "memory")
#define FENCE() asm volatile("" ::: "memory")
#define BAR() do { FENCE(); __builtin_amdgcn_s_barrier(); FENCE(); } while (0)
#define SB() __builtin_amdgcn_sched_barrier(0)
#define RSRC(p, n) __builtin_amdgcn_make_buffer_rsrc((void*)(p), 0, (n), 0x00020000)
#define DMA16(r, dst, voff, soff) __builtin_amdgcn_raw_ptr_buffer_load_lds(r, dst, 16, voff, soff, 0, 0)

__device__ __forceinline__ float bf2f(u16 v) { return __uint_as_float(((unsigned)v) << 16); }
typedef __bf16 bf16x2_t __attribute__((ext_vector_type(2)));
__device__ __forceinline__ unsigned cvt_pk_bf16(float lo, float hi) { return __builtin_bit_cast(unsigned, __builtin_convertvector(((f32x2){ lo, hi }), bf16x2_t)); }
__device__ __forceinline__ float rbf(float f) { return __uint_as_float(cvt_pk_bf16(f, f) << 16); }
__device__ __forceinline__ float lo16(unsigned v) { return __uint_as_float(v << 16); }
__device__ __forceinline__ float hi16(unsigned v) { return __uint_as_float(v & 0xffff0000u); }
__device__ __forceinline__ s16x4 tr_read(LAS const char* p) {
    typedef short v4i16_t __attribute__((ext_vector_type(4)));
    return __builtin_bit_cast(s16x4, __builtin_amdgcn_ds_read_tr16_b64_v4i16((LAS v4i16_t*)p));
}
__device__ __forceinline__ bf16x8 cat8(s16x4 lo, s16x4 hi) { return __builtin_shufflevector(lo, hi, 0, 1, 2, 3, 4, 5, 6, 7); }
#define stamp(st, i) do {} while (0)
#define stamp_flush(st) do {} while (0)
#define STAMP_DECL
#define TS(k) do {} while (0)
template <int CTRL, int RM> __device__ __forceinline__ float dppf(float old, float v) {
    return __int_as_float(__builtin_amdgcn_update_dpp(__float_as_int(old), __float_as_int(v), CTRL, RM, 0xf, false));
}
__device__ __forceinline__ float wave_max_nonneg(float v) {
    v = fmaxf(v, dppf<0x111, 0xf>(0.f, v)); v = fmaxf(v, dppf<0x112, 0xf>(0.f, v));
    v = fmaxf(v, dppf<0x114, 0xf>(0.f, v)); v = fmaxf(v, dppf<0x118, 0xf>(0.f, v));
    v = fmaxf(v, dppf<0x142, 0xa>(0.f, v)); v = fmaxf(v, dppf<0x143, 0xc>(0.f, v));
    return __int_as_float(__builtin_amdgcn_readlane(__float_as_int(v), 63));
}

__device__ __forceinline__ u64 flag_load(char* ws, int idx) { return __hip_atomic_load((GAS u64*)(ws + WS_FLAG) + idx, __ATOMIC_RELAXED, __HIP_MEMORY_SCOPE_AGENT); }
__device__ __forceinline__ void flag_set(char* ws, int idx, u64 fv, int lane) { if (lane == 0) __hip_atomic_store((GAS u64*)(ws + WS_FLAG) + idx, fv, __ATOMIC_RELAXED, __HIP_MEMORY_SCOPE_AGENT); }
__device__ __forceinline__ void flag_wait(char* ws, int idx, u64 first, u64 fv) {
    if (__all(first == fv)) return;
    const u64 t0 = __builtin_amdgcn_s_memrealtime();
    for (;;) {
        const u64 v = flag_load(ws, idx);
        if (__all(v == fv)) return;
        if (__builtin_amdgcn_s_memrealtime() - t0 > 400000ull) return;
        __builtin_amdgcn_s_sleep(1);
    }
}
__device__ __forceinline__ void lds_arrive(LAS char* lds, int word, int lane) {
    WAIT_L(0);
    if (lane == 0) __hip_atomic_fetch_add((LAS unsigned*)(lds + S_SYNC) + word, 1u, __ATOMIC_RELAXED, __HIP_MEMORY_SCOPE_WORKGROUP);
}
__device__ __forceinline__ void lds_wait(LAS char* lds, int word, unsigned n) {
    const u64 t0 = __builtin_amdgcn_s_memrealtime();
    while ((unsigned)__builtin_amdgcn_readfirstlane(*(volatile LAS unsigned*)(lds + S_SYNC + 4 * word)) < n) {
        if (__builtin_amdgcn_s_memrealtime() - t0 > 400000ull) break;
        __builtin_amdgcn_s_sleep(1);
    }
    FENCE();
}

typedef f32x2 cf;
__device__ __forceinline__ cf cmul(cf a, cf b) {
    cf t, r;
    asm("v_pk_mul_f32 %0, %1, %2 op_sel:[1,1] op_sel_hi:[1,0] neg_lo:[0,1]" : "=v"(t) : "v"(a), "v"(b));
    asm("v_pk_fma_f32 %0, %1, %2, %3 op_sel:[0,0,0] op_sel_hi:[0,1,1]" : "=v"(r) : "v"(a), "v"(b), "v"(t));
    return r;
}
template <int N> __device__ __forceinline__ void cmulN(cf (&r)[N], const cf (&a)[N], const cf (&b)[N]) {
    cf t[N];
#pragma unroll
    for (int q = 0; q < N; ++q) asm volatile("v_pk_mul_f32 %0, %1, %2 op_sel:[1,1] op_sel_hi:[1,0] neg_lo:[0,1]" : "=v"(t[q]) : "v"(a[q]), "v"(b[q]));
#pragma unroll
    for (int q = 0; q < N; ++q) asm volatile("v_pk_fma_f32 %0, %1, %2, %3 op_sel:[0,0,0] op_sel_hi:[0,1,1]" : "=v"(r[q]) : "v"(a[q]), "v"(b[q]), "v"(t[q]));
}
template <int OFF, int N> __device__ __forceinline__ void put_rows(unsigned base, const unsigned (&v)[N]) {
    static_assert(N == 8 || N == 9, "");
    unsigned keep;
    if constexpr (N == 8)
        asm volatile("s_mov_b32 %0, m0\n\ts_mov_b32 m0, %1\n\ts_nop 0\n\t"
                     "ds_write_addtid_b32 %2 offset:%c10\n\tds_write_addtid_b32 %3 offset:%c10+288\n\tds_write_addtid_b32 %4 offset:%c10+576\n\tds_write_addtid_b32 %5 offset:%c10+864\n\t"
                     "ds_write_addtid_b32 %6 offset:%c10+1152\n\tds_write_addtid_b32 %7 offset:%c10+1440\n\tds_write_addtid_b32 %8 offset:%c10+1728\n\tds_write_addtid_b32 %9 offset:%c10+2016\n\t"
                     "s_mov_b32 m0, %0"
                     : "=&s"(keep) : "s"(base), "v"(v[0]), "v"(v[1]), "v"(v[2]), "v"(v[3]), "v"(v[4]), "v"(v[5]), "v"(v[6]), "v"(v[7]), "n"(OFF) : "memory");
    else
        asm volatile("s_mov_b32 %0, m0\n\ts_mov_b32 m0, %1\n\ts_nop 0\n\t"
                     "ds_write_addtid_b32 %2 offset:%c11\n\tds_write_addtid_b32 %3 offset:%c11+288\n\tds_write_addtid_b32 %4 offset:%c11+576\n\tds_write_addtid_b32 %5 offset:%c11+864\n\t"
                     "ds_write_addtid_b32 %6 offset:%c11+1152\n\tds_write_addtid_b32 %7 offset:%c11+1440\n\tds_write_addtid_b32 %8 offset:%c11+1728\n\tds_write_addtid_b32 %9 offset:%c11+2016\n\t"
                     "ds_write_addtid_b32 %10 offset:%c11+2304\n\ts_mov_b32 m0, %0"
                     : "=&s"(keep) : "s"(base), "v"(v[0]), "v"(v[1]), "v"(v[2]), "v"(v[3]), "v"(v[4]), "v"(v[5]), "v"(v[6]), "v"(v[7]), "v"(v[8]), "n"(OFF) : "memory");
}
__device__ __forceinline__ cf zpow(float a2, float tr, float p) {
    const float e = __builtin_amdgcn_exp2f(a2 * p), r = __builtin_amdgcn_fractf(tr * p);
    return (cf){ e * __builtin_amdgcn_cosf(r), e * __builtin_amdgcn_sinf(r) };
}
__device__ __forceinline__ unsigned pk_f16(cf a) { f16x2 h = { (_Float16)a.x, (_Float16)a.y }; return __builtin_bit_cast(unsigned, h); }

template <int OFFB>
__device__ __forceinline__ void kgen_task(float a2, float tr, cf C, unsigned mbase, LAS const char* sy, int lane, f32x4 (&tot)[3]) {
    float mx = wave_max_nonneg(fmaxf(fabsf(C.x), fabsf(C.y)));
    int ex = (int)(__float_as_uint(mx) >> 23); ex = ex < 13 ? 13 : ex;
    const float scale = __uint_as_float((unsigned)(267 - ex) << 23), unscale = __uint_as_float((unsigned)(ex - 12) << 23);
    LAS const char* sx = sy + 16 * PRS;
    const int rd = (lane & 15) * PRS + (lane >> 4) * 16;
    const cf z1 = zpow(a2, tr, 1.f), z8 = zpow(a2, tr, 8.f), z128 = zpow(a2, tr, 128.f), z1024 = zpow(a2, tr, 1024.f);
    const cf C0 = C * scale, w8 = { z8.x, -z8.y };
    cf l1[5];
    { const cf a[5] = { z1, z128, w8, C0, C0 }, bq[5] = { z1, z128, w8, z1, z1024 }; cmulN<5>(l1, a, bq); }
    const cf z2 = l1[0], z256 = l1[1], y2 = l1[2], v1 = l1[3], t0 = l1[4];
    cf l2[9];
    { const cf a[9] = { z2, z256, y2, y2, C0, v1, C0, t0, t0 }, bq[9] = { z2, z256, w8, y2, z2, z2, z256, z128, z1024 }; cmulN<9>(l2, a, bq); }
    const cf z4 = l2[0], z512 = l2[1], y3 = l2[2], y4 = l2[3], v2 = l2[4], v3 = l2[5], s0 = l2[6], t1 = l2[7], t8 = l2[8];
    cf l3[13];
    { const cf a[13] = { w8, y2, y3, y4, C0, v1, v2, v3, s0, s0, C0, t0, t1 }, bq[13] = { y4, y4, y4, y4, z4, z4, z4, z4, z2, z4, z512, z256, z256 }; cmulN<13>(l3, a, bq); }
    const cf y8 = l3[3], s1 = l3[8], s2 = l3[9], p0 = l3[10], t2 = l3[11], t3 = l3[12];
    cf l4[7];
    { const cf a[7] = { w8, y2, y3, y4, l3[0], l3[1], l3[2] }, bq[7] = { y8, y8, y8, y8, y8, y8, y8 }; cmulN<7>(l4, a, bq); }
    {
        const unsigned u0[8] = { pk_f16((cf){ 1.f, 0.f }), pk_f16(w8), pk_f16(y2), pk_f16(y3), pk_f16(y4), pk_f16(l3[0]), pk_f16(l3[1]), pk_f16(l3[2]) };
        const unsigned u1[8] = { pk_f16(y8), pk_f16(l4[0]), pk_f16(l4[1]), pk_f16(l4[2]), pk_f16(l4[3]), pk_f16(l4[4]), pk_f16(l4[5]), pk_f16(l4[6]) };
        SB(); put_rows<OFFB, 8>(mbase, u0); put_rows<OFFB + 8 * PRS, 8>(mbase, u1);
    }
    cf l5[8];
    { const cf a[8] = { C0, v1, v2, v3, l3[4], l3[5], l3[6], l3[7] }, bq[8] = { z128, z128, z128, z128, z128, z128, z128, z128 }; cmulN<8>(l5, a, bq); }
    {
        unsigned u0[8] = { pk_f16(C0), pk_f16(v1), pk_f16(v2), pk_f16(v3), pk_f16(l3[4]), pk_f16(l3[5]), pk_f16(l3[6]), pk_f16(l3[7]) }, u1[8];
#pragma unroll
        for (int q = 0; q < 8; ++q) u1[q] = pk_f16(l5[q]);
        SB(); put_rows<OFFB + 16 * PRS, 8>(mbase, u0); put_rows<OFFB + 24 * PRS, 8>(mbase, u1);
    }
    f16x8 yb[4], a0[4], a1[4], a2f[4];
#define RDA(dst) _Pragma("unroll") for (int ks = 0; ks < 4; ++ks) dst[ks] = *(LAS const f16x8*)(sx + rd + ks * 64)
#define MMA(t, A, B) do { f32x4 c_ = { 0, 0, 0, 0 }; _Pragma("unroll") for (int ks = 0; ks < 4; ++ks) c_ = __builtin_amdgcn_mfma_f32_16x16x32_f16(A[ks], B[ks], c_, 0, 0, 0); tot[t] += c_ * unscale; } while (0)
#pragma unroll
    for (int ks = 0; ks < 4; ++ks) yb[ks] = *(LAS const f16x8*)(sy + rd + ks * 64);
    RDA(a0); SB();
    cf l6[4];
    { const cf a[4] = { s1, p0, p0, t0 }, bq[4] = { z4, z4, z128, z512 }; cmulN<4>(l6, a, bq); }
    const cf s3 = l6[0], p1 = l6[1], p2 = l6[2], t4 = l6[3];
    cf l7[8];
    { const cf a[8] = { s0, s1, s2, s3, p1, t1, t2, t3 }, bq[8] = { z128, z128, z128, z128, z128, z512, z512, z512 }; cmulN<8>(l7, a, bq); }
    const cf p3 = l7[4];
    cf l8[4];
    { const cf a[4] = { p0, p1, p2, p3 }, bq[4] = { z256, z256, z256, z256 }; cmulN<4>(l8, a, bq); }
    {
        const unsigned u0[8] = { pk_f16(s0), pk_f16(s1), pk_f16(s2), pk_f16(s3), pk_f16(l7[0]), pk_f16(l7[1]), pk_f16(l7[2]), pk_f16(l7[3]) };
        const unsigned u1[8] = { pk_f16(p0), pk_f16(p1), pk_f16(p2), pk_f16(p3), pk_f16(l8[0]), pk_f16(l8[1]), pk_f16(l8[2]), pk_f16(l8[3]) };
        SB(); put_rows<OFFB + 16 * PRS, 8>(mbase, u0); put_rows<OFFB + 24 * PRS, 8>(mbase, u1);
    }
    RDA(a1); SB();
    {
        const unsigned u2[9] = { pk_f16(t0), pk_f16(t1), pk_f16(t2), pk_f16(t3), pk_f16(t4), pk_f16(l7[5]), pk_f16(l7[6]), pk_f16(l7[7]), pk_f16(t8) };
        SB(); put_rows<OFFB + 16 * PRS, 9>(mbase, u2);
    }
    RDA(a2f); SB();
    MMA(0, a0, yb); MMA(1, a1, yb); MMA(2, a2f, yb);
#undef RDA
#undef MMA
}
__device__ __forceinline__ float fexp(float v) { return __builtin_amdgcn_exp2f(v * 1.44269504089f); }
struct ModeP { float a2, tr; cf C; };
__device__ __forceinline__ ModeP mode_setup(u16 la, u16 ai, u16 cr, u16 ci, float dt) {
    const float ar = -rbf(fexp(bf2f(la)));
    return { (ar * dt) * 1.44269504089f, (bf2f(ai) * dt) * 0.15915494309f, (cf){ bf2f(cr) * dt, bf2f(ci) * dt } };
}

struct Frags { bf16x8 x[4], g[3], u[3]; };
__global__ __launch_bounds__(512, 2) void k_fused(const u16* __restrict__ x, const u16* __restrict__ logA, const u16* __restrict__ Aim, const u16* __restrict__ Cre,
                                                  const u16* __restrict__ Cim, const u16* __restrict__ logdt, const u16* __restrict__ Dskip,
                                                  const u16* __restrict__ wg, const u16* __restrict__ wu, const u16* __restrict__ wd, const u16* __restrict__ gw,
                                                  char* __restrict__ ws, u16* __restrict__ out) {
    extern __shared__ __attribute__((aligned(16))) char smem[];
    LAS char* lds = (LAS char*)smem;
    const int tid = threadIdx.x, wid = __builtin_amdgcn_readfirstlane(tid >> 6), lane = tid & 63;
    const int b = blockIdx.x, xcd = b & 7, j = b >> 3;
    const int tm = 8 * (xcd & 1) + (j & 7), tn = 4 * (xcd >> 1) + (j >> 3);
    const int row0 = tm * 128, f0 = tn * 96;
    const bool conv = b < 160;
    const int w = wid & 3, d = 4 * b + w;
    const unsigned mbase = (unsigned)(32768 + 9216 * w);
    u64* st = (u64*)(ws + WS_STAMP) + (size_t)(b * 8 + wid) * 32;
    STAMP_DECL;
    stamp(st, 0);
    const unsigned epoch_v = __hip_atomic_load((GAS unsigned*)(ws + WS_EPOCH), __ATOMIC_RELAXED, __HIP_MEMORY_SCOPE_AGENT);
    unsigned T; u64 fv;
#define GET_TAG() do { T = (unsigned)__builtin_amdgcn_readfirstlane(epoch_v) + 1u; fv = ((u64)T << 32) | (unsigned)~T; } while (0)

    if (wid >= 4) {
        u16 pA[2], pI[2], pR[2], pC[2], pdt = 0, pds = 0;
        if (conv) {
#pragma unroll
            for (int i = 0; i < 2; ++i) { const int o = d * NM + 64 * (i + 1) + lane; pA[i] = logA[o]; pI[i] = Aim[o]; pR[i] = Cre[o]; pC[i] = Cim[o]; }
            pdt = logdt[d]; pds = Dskip[d];
        }
        ModeP mp[2];
        f32x4 tot[3];
        u64 fu0 = 0; const int fui = FU + (d / 80) * 64 + lane;
#define MODE_SETUP() do { if (conv) { const float dt_ = rbf(fexp(bf2f(pdt))); mp[0] = mode_setup(pA[0], pI[0], pR[0], pC[0], dt_); mp[1] = mode_setup(pA[1], pI[1], pR[1], pC[1], dt_); } } while (0)
#define PART_READ() do { if (conv) { _Pragma("unroll") for (int t_ = 0; t_ < 3; ++t_) tot[t_] = *(LAS const f32x4*)(lds + S_PART + w * 3072 + t_ * 1024 + lane * 16); fu0 = flag_load(ws, fui); } } while (0)
        if (w < 2) {
            auto rx = RSRC(x, L * D * 2); auto rg = RSRC(wg, D * F * 2); auto ru = RSRC(wu, D * F * 2);
            const int vx = (row0 + 64 * w + (lane >> 3)) * 1280 + (((lane & 7) ^ (lane >> 3)) << 4);
            int vw[6];
#pragma unroll
            for (int i = 0; i < 6; ++i) { const int P = 64 * (6 * w + i) + lane, k = P / 12, cp = P % 12, c = cp ^ ((k & 8) >> 2); vw[i] = k * 3072 + f0 * 2 + c * 16; }
            auto issue = [&](int kt, int slot) {
                LAS char* sb = lds + slot * STAGE;
#pragma unroll
                for (int i = 0; i < 8; ++i) DMA16(rx, sb + XOFF + (8 * w + i) * 1024, vx, kt * 128 + i * 10240);
#pragma unroll
                for (int i = 0; i < 6; ++i) {
                    DMA16(rg, sb + GOFF + (6 * w + i) * 1024, vw[i], kt * 64 * 3072);
                    DMA16(ru, sb + UOFF + (6 * w + i) * 1024, vw[i], kt * 64 * 3072);
                }
            };
            issue(0, 0); issue(1, 1);
            WAIT_V(20);
            GET_TAG();
            BAR();
            issue(2, 2);
            SB(); MODE_SETUP(); SB();
            int slot = 0;
#pragma unroll 1
            for (int t = 1; t < NKT - 2; ++t) {
                TS(3 * t);
                WAIT_V(20);
                TS(3 * t + 1);
                BAR();
                issue(t + 2, slot);
                TS(3 * t + 2);
                slot = slot == NSLOT - 1 ? 0 : slot + 1;
            }
            WAIT_V(20); BAR();
            PART_READ();
            WAIT_V(0); WAIT_L(0); BAR();
        } else {
            const int par = w & 1, l0 = (b >> 3) * 64, d0 = (b & 7) * 80;
            if (par && lane < 8) *(LAS unsigned*)(lds + S_SYNC + 4 * lane) = 0u;
            WAIT_L(0);
            GET_TAG();
            BAR();
            if (!par) {
                auto rx = RSRC(x, L * D * 2);
#pragma unroll
                for (int i = 0; i < 11; ++i) {
                    const int P = 64 * i + lane, r = P / 10, c = P % 10, l = l0 - 1 + r;
                    DMA16(rx, lds + P_U + i * 1024, (l >= 0 && r < 65) ? (l * D + d0) * 2 + c * 16 : 0x40000000, 0);
                }
            }
            MODE_SETUP();
            BAR();
            if (!par) { WAIT_V(0); if (l0 == 0 && lane < 10) *(LAS u32x4*)(lds + P_U + lane * 16) = (u32x4){0, 0, 0, 0}; WAIT_L(0); }
            BAR();
            auto rur = RSRC(ws + WS_UR, 2 * D * URS * 2);
            const int g = lane >> 4, q = (lane & 15) >> 2, p = lane & 3, Tt = 1984 - l0;
            LAS const char* pr0 = lds + P_U + (64 - par - 8 * (4 * (g & 1) + p) - q) * 160 + (g >> 1) * 8;
            const int so0 = ((par * D + d0 + 4 * (g >> 1) + (lane & 3)) * URS + Tt + 8 * (4 * (g & 1) + ((lane & 15) >> 2))) * 2;
#pragma unroll 1
            for (int e = 0; e < 2; ++e) {
#pragma unroll
                for (int cg = 0; cg < 5; ++cg) {
                    LAS const char* pr = pr0 + (5 * e + cg) * 16;
                    const bf16x8 v = cat8(tr_read(pr), tr_read(pr - 4 * 160));
                    __builtin_amdgcn_raw_buffer_store_b128(__builtin_bit_cast(u32x4, v), rur, so0, (5 * e + cg) * 8 * URS * 2, SC1);
                }
                BAR();
            }
            if (l0 == 0) {
#pragma unroll
                for (int i = 0; i < 2; ++i) {
                    const int P = 64 * i + lane;
                    __builtin_amdgcn_raw_buffer_store_b128((u32x4){0, 0, 0, 0}, rur, P < 80 ? ((par * D + d0 + P) * URS + 2048) * 2 : 0x40000000, 0, SC1);
                }
            }
            BAR(); BAR();
            WAIT_V(0);
            flag_set(ws, FU + ((b & 7) * 32 + (b >> 3)) * 2 + par, fv, lane);
            BAR(); BAR();
            PART_READ();
            WAIT_L(0); BAR();
        }
        stamp(st, 1);
        if (!conv) {
            lds_arrive(lds, 0, lane);
            if (b == 255 && w == 3) {
                const u64 t0 = __builtin_amdgcn_s_memrealtime();
                for (;;) {
                    bool ok = true;
#pragma unroll
                    for (int i = 0; i < 16; ++i) ok &= flag_load(ws, FH + i * 64 + lane) == fv;
                    if (__all(ok) || __builtin_amdgcn_s_memrealtime() - t0 > 400000ull) break;
                    __builtin_amdgcn_s_sleep(8);
                }
                if (lane == 0) __hip_atomic_store((GAS unsigned*)(ws + WS_EPOCH), T, __ATOMIC_RELAXED, __HIP_MEMORY_SCOPE_AGENT);
            }
            stamp_flush(st);
            return;
        }
        LAS char* wl = lds + S_PER + w * PER_W;
        LAS char* sy = lds + S_SCR + w * SCR_W;
        stamp(st, 8);
        kgen_task<8192>(mp[0].a2, mp[0].tr, mp[0].C, mbase, sy, lane, tot);
        flag_wait(ws, fui, fu0, fv);
        {
            auto rur = RSRC(ws + WS_UR, 2 * D * URS * 2);
#pragma unroll
            for (int c = 0; c < 4; ++c)
#pragma unroll
                for (int q = 0; q < 4; ++q)
                    __builtin_amdgcn_raw_ptr_buffer_load_lds(rur, wl + c * URC + q * 1024, 16, lane * 16, (((c & 1) * D + d) * URS + (c & 2)) * 2 + q * 1024, 0, SC1);
        }
        SB();
        stamp(st, 9);
        kgen_task<8192>(mp[1].a2, mp[1].tr, mp[1].C, mbase, sy, lane, tot);
        const float dsk = bf2f(pds);
        stamp(st, 2);
        {
            const int bq = lane & 15, g = lane >> 4;
            LAS char* kx = wl + S_KX; LAS char* kc = sy;
            *(LAS u32x2*)(kx + lane * 8) = (u32x2){0, 0};
            {
                const unsigned w0 = cvt_pk_bf16(tot[1][0], tot[1][1]), w1 = cvt_pk_bf16(tot[1][2], tot[1][3]);
                const int q0 = g < 2 ? 64 * g + 4 * bq : 128 + 64 * (g - 2) + 2 * bq, q1 = g < 2 ? q0 + 2 : q0 + 32;
                *(LAS unsigned*)(kc + q0 * 2) = w0; *(LAS unsigned*)(kc + q1 * 2) = w1;
                const unsigned x0 = cvt_pk_bf16(tot[2][0], tot[2][1]), x1 = cvt_pk_bf16(tot[2][2], tot[2][3]);
                LAS u16* k2 = (LAS u16*)(kc + (256 + 64 * g + bq) * 2);
                k2[0] = (u16)x0; k2[16] = (u16)(x0 >> 16); k2[32] = (u16)x1; k2[48] = (u16)(x1 >> 16);
            }
            {
                const int p0 = (g < 2 ? 0 : 128) + 8 * bq + 4 * (g & 1);
                *(LAS u32x2*)(kx + (256 + p0) * 2) = (u32x2){ cvt_pk_bf16(tot[0][0], tot[0][1]), cvt_pk_bf16(tot[0][2], tot[0][3]) };
            }
            FENCE();
            const int q1 = 4 * (lane & 31), q2 = 128 + 2 * lane, q3 = 256 + lane;
            const u32x2 w1v = *(LAS const u32x2*)(kc + q1 * 2); const unsigned v4 = *(LAS const u16*)(kc + (q1 + 4) * 2);
            const unsigned w2v = *(LAS const unsigned*)(kc + q2 * 2), c2 = *(LAS const u16*)(kc + (q2 + 2) * 2);
            const unsigned A0 = *(LAS const u16*)(kc + q3 * 2), B0 = *(LAS const u16*)(kc + (q3 + 1) * 2);
            const unsigned A1 = *(LAS const u16*)(kc + (q3 + 64) * 2), B1 = *(LAS const u16*)(kc + (q3 + 65) * 2);
            if (lane < 32)
                *(LAS u32x4*)(kx + (256 + 8 * (32 + lane)) * 2) = (u32x4){ __builtin_amdgcn_perm(w1v.x, w1v.x, 0x01000100), __builtin_amdgcn_perm(w1v.y, w1v.x, 0x05040302),
                                                                         __builtin_amdgcn_perm(w1v.y, w1v.y, 0x01000100), __builtin_amdgcn_perm(v4, w1v.y, 0x05040302) };
            *(LAS u32x4*)(kx + (256 + 8 * (64 + lane)) * 2) = (u32x4){ __builtin_amdgcn_perm(w2v, w2v, 0x01000100), w2v, __builtin_amdgcn_perm(w2v, w2v, 0x03020302), __builtin_amdgcn_perm(c2, c2, 0x01000100) };
            { const unsigned AA = A0 | (A0 << 16), BB = B0 | (B0 << 16);
              *(LAS u32x4*)(kx + (256 + 8 * (128 + lane)) * 2) = (u32x4){ AA, AA, (lane & 1) ? BB : (A0 | (B0 << 16)), BB }; }
            { const unsigned AA = A1 | (A1 << 16), BB = B1 | (B1 << 16);
              *(LAS u32x4*)(kx + (256 + 8 * (192 + lane)) * 2) = (u32x4){ AA, AA, (lane & 1) ? BB : (A1 | (B1 << 16)), BB }; }
            if (lane < 8) *(LAS u32x4*)(wl + (lane >> 1) * URC + 4096 + (lane & 1) * 16) = (u32x4){0, 0, 0, 0};
        }
        lds_arrive(lds, 0, lane);
        WAIT_V(0);
        __builtin_amdgcn_s_setprio(2);
        stamp(st, 3);
        f32x4 y[8];
#pragma unroll
        for (int c = 0; c < 8; ++c) y[c] = (f32x4){0, 0, 0, 0};
        {
            const int r = lane & 15, kq = lane >> 4;
            LAS const char* ab = wl + (3 - (r & 3)) * URC + (511 - 8 * 63 - 4 * (kq >> 1) + 2 * (kq & 1) - (r >> 2)) * 8;
            LAS const char* bbp = wl + S_KX + 512 + 32 * r - 32 * (kq >> 1) + 16 * (kq & 1) - 64 * 7;
            bf16x8 A[2][8], B[2][8];
            auto ldab = [&](int set, int rho, int mu) {
                const int M = 8 * mu + rho;
                const s16x4 lo = *(volatile LAS const s16x4*)(ab + (63 - M) * 64), hi = *(volatile LAS const s16x4*)(ab + (63 - M) * 64 + 8);
                A[set][mu] = cat8(lo, hi);
                B[set][mu] = *(volatile LAS const bf16x8*)(bbp + (8 * mu - rho + 7) * 64);
            };
#pragma unroll
            for (int mu = 0; mu < 8; ++mu) ldab(0, 0, mu);
#pragma unroll
            for (int rho = 0; rho < 8; ++rho) {
                const int cur = rho & 1;
                int e = 0;
#pragma unroll
                for (int mu = 0; mu < 8; ++mu)
#pragma unroll
                    for (int c = mu; c < 8; ++c) {
                        y[c] = __builtin_amdgcn_mfma_f32_16x16x32_bf16(A[cur][mu], B[cur][c - mu], y[c], 0, 0, 0);
                        ++e;
                        if (rho < 7 && (e & 3) == 0 && e <= 32) { SB(); ldab(cur ^ 1, rho + 1, e / 4 - 1); SB(); }
                    }
            }
        }
        stamp(st, 4);
        {
            const int col = lane & 15, g = lane >> 4;
            auto rat = RSRC(ws + WS_AT, D * L * 2);
            u32x2 uu[8];
#pragma unroll
            for (int c = 0; c < 8; ++c) uu[c] = *(LAS const u32x2*)(wl + (2044 - (256 * c + 16 * col + 4 * g)) * 2);
#pragma unroll
            for (int c = 0; c < 8; ++c) {
                const int l = 256 * c + 16 * col + 4 * g;
                const unsigned y01 = cvt_pk_bf16(y[c][0], y[c][1]), y23 = cvt_pk_bf16(y[c][2], y[c][3]);
                const unsigned s01 = cvt_pk_bf16(hi16(uu[c].y) * dsk, lo16(uu[c].y) * dsk), s23 = cvt_pk_bf16(hi16(uu[c].x) * dsk, lo16(uu[c].x) * dsk);
                const u32x2 o = { cvt_pk_bf16(lo16(y01) + lo16(s01), hi16(y01) + hi16(s01)), cvt_pk_bf16(lo16(y23) + lo16(s23), hi16(y23) + hi16(s23)) };
                __builtin_amdgcn_raw_buffer_store_b64(o, rat, (d * L + l) * 2, 0, SC1);
            }
            WAIT_V(0);
            flag_set(ws, FA + d, fv, lane);
        }
        stamp(st, 5);
        stamp_flush(st);
        return;
    }

    if (conv) {
        const int o = d * NM + lane;
        const u16 pA = logA[o], pI = Aim[o], pR = Cre[o], pC = Cim[o], pdt = logdt[d];
        const ModeP m0 = mode_setup(pA, pI, pR, pC, rbf(fexp(bf2f(pdt))));
        f32x4 tot[3] = { {0, 0, 0, 0}, {0, 0, 0, 0}, {0, 0, 0, 0} };
        kgen_task<49152>(m0.a2, m0.tr, m0.C, mbase, lds + 81920 + w * 9216, lane, tot);
#pragma unroll
        for (int t = 0; t < 3; ++t) *(LAS f32x4*)(lds + S_PART + w * 3072 + t * 1024 + lane * 16) = tot[t];
        WAIT_L(0);
    } else if (b < 192) {
        const int task = (b - 160) * 4 + w;
        const int part = lane & 7;
        u32x4 gv[10], xv[2][10];
#pragma unroll
        for (int i = 0; i < 10; ++i) {
            gv[i] = *(const u32x4*)(gw + 64 * i + 8 * part);
            xv[0][i] = *(const u32x4*)(x + (16 * task + (lane >> 3)) * D + 64 * i + 8 * part);
            xv[1][i] = *(const u32x4*)(x + (16 * task + 8 + (lane >> 3)) * D + 64 * i + 8 * part);
        }
#pragma unroll
        for (int h = 0; h < 2; ++h) {
            float sum = 0.f;
#pragma unroll
            for (int i = 0; i < 10; ++i)
#pragma unroll
                for (int q = 0; q < 4; ++q) { const unsigned pr = cvt_pk_bf16(lo16(xv[h][i][q]) * lo16(gv[i][q]), hi16(xv[h][i][q]) * hi16(gv[i][q])); sum += lo16(pr); sum += hi16(pr); }
            sum += dppf<0xB1, 0xf>(0.f, sum); sum += dppf<0x4E, 0xf>(0.f, sum); sum += dppf<0x141, 0xf>(0.f, sum);
            const float z = rbf(sum), gt = rbf(1.f / (1.f + __expf(-z)));
            if (part == 0) __hip_atomic_store((GAS unsigned*)(ws + WS_G) + 16 * task + 8 * h + (lane >> 3), __float_as_uint(gt), __ATOMIC_RELAXED, __HIP_MEMORY_SCOPE_AGENT);
        }
    } else if (b >= 208) {
        const int task = (b - 208) * 4 + w, tc = task & 7, sp = task >> 3;
        LAS char* pw = lds + 81920 + w * 10240;
        auto rwd = RSRC(wd, F * D * 2);
#pragma unroll
        for (int i = 0; i < 10; ++i) { const int P = 64 * i + lane, r = P / 10, c = P % 10; DMA16(rwd, pw + i * 1024, ((64 * sp + r) * D + tc * 80) * 2 + c * 16, 0); }
        WAIT_V(0);
        auto rwf = RSRC(ws + WS_WDF, F * D * 2);
        const int g = lane >> 4, q = (lane & 15) >> 2, p = lane & 3;
#pragma unroll
        for (int s2 = 0; s2 < 2; ++s2)
#pragma unroll
            for (int i5 = 0; i5 < 5; ++i5) {
                const int dc = i5 < 4 ? 32 * (i5 >> 1) + 8 * p + 4 * (i5 & 1) : 64 + 4 * p;
                LAS const char* pr = pw + (32 * s2 + 4 * g + q) * 160 + dc * 2;
                const bf16x8 v = cat8(tr_read(pr), tr_read(pr + 16 * 160));
                __builtin_amdgcn_raw_buffer_store_b128(__builtin_bit_cast(u32x4, v), rwf, ((tc * 5 + i5) * 48 + 2 * sp + s2) * 1024 + lane * 16, 0, SC1);
            }
        WAIT_L(0);
    }
    stamp(st, 8);
    const int m = wid >> 1, n = wid & 1;
    const int tr2 = 8 * (xcd >> 1) + (j & 7), tc2 = 4 * (xcd & 1) + (j >> 3);
    f32x4 ag[4][3], au[4][3];
    u64 fw0 = 0;
    const int fwi = FW + tc2 * 32 + 6 * wid + (lane % 6);
    {
        const int kq = lane >> 4, q = (lane & 15) >> 2, p = lane & 3, bb = kq & 1;
        const int xa0 = XOFF + (64 * m + (lane & 15)) * 128 + ((kq ^ (lane & 7)) << 4);
        const int xa1 = xa0 ^ 64;
        const int wrow = GOFF + (8 * kq + q) * 192 + (p & 1) * 8 + (p >> 1) * 16 + n * 96;
        const int waE = wrow + bb * 32, waO = wrow - bb * 32;
        const int wa0 = (n & 1) ? waO : waE, wa1 = (n & 1) ? waE : waO;
#pragma unroll
        for (int i = 0; i < 4; ++i)
#pragma unroll
            for (int t = 0; t < 3; ++t) { ag[i][t] = (f32x4){0, 0, 0, 0}; au[i][t] = (f32x4){0, 0, 0, 0}; }
        Frags P, Q;
        auto rd = [&](Frags& Fr, LAS const char* sb, int ks, int r) {
            if (r < 2) {
                Fr.x[2 * r] = *(LAS const bf16x8*)(sb + (ks ? xa1 : xa0) + (2 * r) * 2048);
                Fr.x[2 * r + 1] = *(LAS const bf16x8*)(sb + (ks ? xa1 : xa0) + (2 * r + 1) * 2048);
            } else {
                const int tt = (r - 2) >> 1, up = (r - 2) & 1;
                LAS const char* pw = sb + ((tt & 1) ? wa1 : wa0) + tt * 32 + ks * 6144 + up * (UOFF - GOFF);
                const bf16x8 v = cat8(tr_read(pw), tr_read(pw + 768));
                if (up) Fr.u[tt] = v; else Fr.g[tt] = v;
            }
        };
        auto mm = [&](const Frags& Fr, int r) {
#pragma unroll
            for (int i = 0; i < 3; ++i) {
                const int e = 3 * r + i, tt = e >> 3, up = (e >> 2) & 1, mt = e & 3;
                if (up) au[mt][tt] = __builtin_amdgcn_mfma_f32_16x16x32_bf16(Fr.u[tt], Fr.x[mt], au[mt][tt], 0, 0, 0);
                else ag[mt][tt] = __builtin_amdgcn_mfma_f32_16x16x32_bf16(Fr.g[tt], Fr.x[mt], ag[mt][tt], 0, 0, 0);
            }
        };
        GET_TAG();
        BAR();
        stamp(st, 1);
#pragma unroll
        for (int r = 0; r < 8; ++r) rd(P, lds, 0, r);
        SB();
#pragma unroll
        for (int r = 0; r < 8; ++r) { rd(Q, lds, 1, r); SB(); mm(P, r); SB(); }
        WAIT_L(0);
        if (!conv) {
            WAIT_V(0);
            if (b < 192) flag_set(ws, FG + (b - 160) * 4 + w, fv, lane);
            else if (b >= 208) { const int task = (b - 208) * 4 + w; flag_set(ws, FW + (task & 7) * 32 + (task >> 3), fv, lane); }
        }
        int slot = 0;
#pragma unroll 1
        for (int t = 1; t < NKT; ++t) {
            TS(2 * t);
            BAR();
            TS(2 * t + 1);
            slot = slot == NSLOT - 1 ? 0 : slot + 1;
            LAS const char* sb = lds + slot * STAGE;
            if (t == NKT - 1) fw0 = flag_load(ws, fwi);
#pragma unroll
            for (int r = 0; r < 8; ++r) { mm(Q, r); SB(); rd(P, sb, 0, r); SB(); }
#pragma unroll
            for (int r = 0; r < 8; ++r) { rd(Q, sb, 1, r); SB(); mm(P, r); SB(); }
            WAIT_L(0);
        }
#pragma unroll
        for (int r = 0; r < 8; ++r) mm(Q, r);
    }
    lds_arrive(lds, 0, lane);
    __builtin_amdgcn_s_setprio(1);
    stamp(st, 2);
    constexpr int WDEP = 5, WPRE = 4;
    auto rw = RSRC(ws + WS_WDF, F * D * 2);
    const int vh = lane * 16, wb = (tc2 * 5 * 48 + 12 * wid) * 1024;
    u32x4 wf[WDEP][5];
    auto ldw = [&](int s) {
#pragma unroll
        for (int i = 0; i < 5; ++i) wf[s % WDEP][i] = __builtin_amdgcn_raw_buffer_load_b128(rw, vh, wb + (i * 48 + s) * 1024, SC1);
    };
    flag_wait(ws, fwi, fw0, fv);
    SB();
#pragma unroll
    for (int s = 0; s < WPRE; ++s) ldw(s);
    SB();
    {
        auto rh = RSRC(ws + WS_H, L * F * 2);
        const int tp0 = (row0 + 64 * m) / 32, ft0 = (f0 + 48 * n) / 16;
#pragma unroll
        for (int ip = 0; ip < 2; ++ip)
#pragma unroll
            for (int tt = 0; tt < 3; ++tt) {
                u32x4 o;
#pragma unroll
                for (int e = 0; e < 2; ++e) {
                    const f32x4 g = ag[2 * ip + e][tt], u = au[2 * ip + e][tt];
#pragma unroll
                    for (int r = 0; r < 2; ++r) {
                        const f32x2 gg = { g[2 * r], g[2 * r + 1] }, uu = { u[2 * r], u[2 * r + 1] };
                        const f32x2 a = gg * -1.44269504089f;
                        f32x2 ex = { __builtin_amdgcn_exp2f(a.x), __builtin_amdgcn_exp2f(a.y) };
                        ex += 1.f;
                        const f32x2 rc = { __builtin_amdgcn_rcpf(ex.x), __builtin_amdgcn_rcpf(ex.y) };
                        const f32x2 h = (gg * uu) * rc;
                        o[2 * e + r] = cvt_pk_bf16(h.x, h.y);
                    }
                }
                __builtin_amdgcn_raw_buffer_store_b128(o, rh, (((tp0 + ip) * 96 + ft0 + tt) * 64 + lane) * 16, 0, SC1);
            }
        stamp(st, 3);
        WAIT_V(0);
        flag_set(ws, FH + ((tm * 2 + m) * 16 + tn) * 2 + n, fv, lane);
        __builtin_amdgcn_s_setprio(0);
        stamp(st, 11);
    }
    SB();
#pragma unroll
    for (int s = WPRE; s < WDEP; ++s) ldw(s);
    f32x4 acc[5][4];
#pragma unroll
    for (int i = 0; i < 5; ++i)
#pragma unroll
        for (int mt = 0; mt < 4; ++mt) acc[i][mt] = (f32x4){0, 0, 0, 0};
    {
        const int fhi = FH + (tr2 * 16 + 4 * wid) * 2 + (lane & 7);
        flag_wait(ws, fhi, flag_load(ws, fhi), fv);
        stamp(st, 4);
        auto rh = RSRC(ws + WS_H, L * F * 2);
        const int hb = (tr2 * 2 * 96 + 24 * wid) * 1024;
        u32x4 hf[3][4];
        auto ldh = [&](int s) {
            hf[s % 3][0] = __builtin_amdgcn_raw_buffer_load_b128(rh, vh, hb + (2 * s) * 1024, SC1);
            hf[s % 3][1] = __builtin_amdgcn_raw_buffer_load_b128(rh, vh, hb + (2 * s + 1) * 1024, SC1);
            hf[s % 3][2] = __builtin_amdgcn_raw_buffer_load_b128(rh, vh, hb + (96 + 2 * s) * 1024, SC1);
            hf[s % 3][3] = __builtin_amdgcn_raw_buffer_load_b128(rh, vh, hb + (96 + 2 * s + 1) * 1024, SC1);
        };
        ldh(0); ldh(1);
        SB();
#pragma unroll
        for (int s = 0; s < 12; ++s) {
            if (s + 2 < 12) ldh(s + 2);
            SB();
            bf16x8 hb4[4];
#pragma unroll
            for (int pr = 0; pr < 2; ++pr) {
                const u32x4 a = hf[s % 3][2 * pr], c = hf[s % 3][2 * pr + 1];
                hb4[2 * pr] = __builtin_bit_cast(bf16x8, (u32x4){ a.x, a.y, c.x, c.y });
                hb4[2 * pr + 1] = __builtin_bit_cast(bf16x8, (u32x4){ a.z, a.w, c.z, c.w });
            }
#pragma unroll
            for (int i = 0; i < 5; ++i)
#pragma unroll
                for (int mt = 0; mt < 4; ++mt)
                    acc[i][mt] = __builtin_amdgcn_mfma_f32_16x16x32_bf16(__builtin_bit_cast(bf16x8, wf[s % WDEP][i]), hb4[mt], acc[i][mt], 0, 0, 0);
            SB();
            if (s + WDEP < 12) ldw(s + WDEP);
            SB();
        }
    }
    stamp(st, 5);
    const int tok = lane & 15, g4 = lane >> 4, l = tr2 * 64 + 16 * wid + tok;
    float gt;
    {
        const int pc = wid + 4 * (lane >> 3);
        const int fai = lane < 24 && pc < 10 ? FA + 80 * tc2 + 8 * pc + (lane & 7) : FG + 4 * tr2 + wid;
        flag_wait(ws, fai, flag_load(ws, fai), fv);
        gt = __uint_as_float(__hip_atomic_load((GAS unsigned*)(ws + WS_G) + l, __ATOMIC_RELAXED, __HIP_MEMORY_SCOPE_AGENT));
        lds_wait(lds, 0, 8);
        auto rat = RSRC(ws + WS_AT, D * L * 2);
#pragma unroll
        for (int i = 0; i < 3; ++i) {
            const int pi = wid + 4 * i;
            if (pi < 10)
                __builtin_amdgcn_raw_ptr_buffer_load_lds(rat, lds + S_AST + pi * 1024, 16, ((tc2 * 80 + 8 * pi + (lane >> 3)) * L + tr2 * 64) * 2 + (((lane & 7) ^ (lane >> 3)) << 4), 0, 0, SC1);
        }
    }
    stamp(st, 6);
#pragma unroll
    for (int mt = 0; mt < 4; ++mt)
        if (mt != wid) {
            const int sl = (wid - mt + 3) & 3;
#pragma unroll
            for (int i = 0; i < 5; ++i) *(LAS f32x4*)(lds + ((mt * 3 + sl) * 5 + i) * 1024 + lane * 16) = acc[i][mt];
        }
    WAIT_V(0);
    stamp(st, 12);
    lds_arrive(lds, 1, lane);
    lds_wait(lds, 1, 4);
    stamp(st, 13);
    {
        const float og = rbf(1.f - gt);
        const int q = tok >> 2, p = tok & 3;
        unsigned o[10];
#pragma unroll
        for (int i = 0; i < 5; ++i) {
            f32x4 s = wid == 0 ? acc[i][0] : wid == 1 ? acc[i][1] : wid == 2 ? acc[i][2] : acc[i][3];
#pragma unroll
            for (int sl = 0; sl < 3; ++sl) s += *(LAS const f32x4*)(lds + ((wid * 3 + sl) * 5 + i) * 1024 + lane * 16);
            const int drow = (i < 4 ? 32 * (i >> 1) + 8 * g4 + 4 * (i & 1) : 64 + 4 * g4) + q;
            const u32x2 a2 = __builtin_bit_cast(u32x2, tr_read(lds + S_AST + drow * 128 + (((2 * wid + (p >> 1)) ^ (drow & 7)) << 4) + (p & 1) * 8));
            const unsigned b01 = cvt_pk_bf16(s[0], s[1]), b23 = cvt_pk_bf16(s[2], s[3]);
            const unsigned ga01 = cvt_pk_bf16(gt * lo16(a2.x), gt * hi16(a2.x)), ga23 = cvt_pk_bf16(gt * lo16(a2.y), gt * hi16(a2.y));
            const unsigned ob01 = cvt_pk_bf16(og * lo16(b01), og * hi16(b01)), ob23 = cvt_pk_bf16(og * lo16(b23), og * hi16(b23));
            o[2 * i] = cvt_pk_bf16(lo16(ga01) + lo16(ob01), hi16(ga01) + hi16(ob01));
            o[2 * i + 1] = cvt_pk_bf16(lo16(ga23) + lo16(ob23), hi16(ga23) + hi16(ob23));
        }
        auto ro = RSRC(out, L * D * 2);
        const int ob = (l * D + tc2 * 80) * 2;
        __builtin_amdgcn_raw_buffer_store_b128((u32x4){ o[0], o[1], o[2], o[3] }, ro, ob + 16 * g4, 0, 0);
        __builtin_amdgcn_raw_buffer_store_b128((u32x4){ o[4], o[5], o[6], o[7] }, ro, ob + 64 + 16 * g4, 0, 0);
        __builtin_amdgcn_raw_buffer_store_b64((u32x2){ o[8], o[9] }, ro, ob + 128 + 8 * g4, 0, 0);
    }
    stamp(st, 7);
    stamp_flush(st);
}

extern "C" void kernel_launch(void* const* d_in, const int* in_sizes, int n_in, void* d_out, int out_size, void* d_ws, size_t ws_size, hipStream_t stream) {
    (void)hipFuncSetAttribute((const void*)k_fused, hipFuncAttributeMaxDynamicSharedMemorySize, LDS_BYTES);
    k_fused<<<256, 512, LDS_BYTES, stream>>>((const u16*)d_in[0], (const u16*)d_in[1], (const u16*)d_in[2], (const u16*)d_in[3], (const u16*)d_in[4], (const u16*)d_in[5],
                                             (const u16*)d_in[6], (const u16*)d_in[8], (const u16*)d_in[9], (const u16*)d_in[10], (const u16*)d_in[11], (char*)d_ws, (u16*)d_out);
}
```

```cpp
#define CONV_PRIO 1
#include <hip/hip_runtime.h>
typedef unsigned short u16;
typedef unsigned long long u64;
typedef short bf16x8 __attribute__((ext_vector_type(8)));
typedef _Float16 f16x8 __attribute__((ext_vector_type(8)));
typedef _Float16 f16x2 __attribute__((ext_vector_type(2)));
typedef short s16x4 __attribute__((ext_vector_type(4)));
typedef float f32x4 __attribute__((ext_vector_type(4)));
typedef float f32x2 __attribute__((ext_vector_type(2)));
typedef unsigned u32x4 __attribute__((ext_vector_type(4)));
typedef unsigned u32x2 __attribute__((ext_vector_type(2)));
typedef unsigned u32x4a4 __attribute__((ext_vector_type(4), aligned(4)));
#define LAS __attribute__((address_space(3)))
#define GAS __attribute__((address_space(1)))
constexpr int L = 2048, D = 640, NM = 192, F = 1536;

constexpr size_t WS_H = 0, WS_WDF = 8u << 20, WS_UR = 12u << 20, WS_AT = 20u << 20, WS_G = 24u << 20, WS_FLAG = 25u << 20, WS_KP = 26u << 20, WS_EPOCH = WS_FLAG + 65536, WS_STAMP = 128u << 20;
constexpr int URS = 2112;
constexpr int FH = 0, FU = 1024, FW = 1536, FG = 1792, FA = 2048, FP = 2688;
constexpr int SC1 = 16;

constexpr int STAGE = 40960, XOFF = 0, GOFF = 16384, UOFF = 28672, NSLOT = 3, NKT = 10;
constexpr int PRS = 288, KSCR = 81920, KSCR_W = 16 * PRS;
constexpr int P_U = 122880, P_W = 134144;
constexpr int S_KX = 145408, KX_W = 4608;
constexpr int URC = 4160, S_UR = 81920, UR_W = 2 * URC;
constexpr int S_AST = 61440;
constexpr int S_SYNC = 145376;
constexpr int Y_ARR0 = 0, Y_ARR1 = 1, Y_ARR2 = 2;
constexpr int S_KC = 76800;
constexpr int LDS_BYTES = 163840;

#define WAIT_V(n) asm volatile("s_waitcnt vmcnt(" #n ")" ::: "memory")
#define WAIT_L(n) asm volatile("s_waitcnt lgkmcnt(" #n ")" ::: "memory")
#define FENCE() asm volatile("" ::: "memory")
#define BAR() do { FENCE(); __builtin_amdgcn_s_barrier(); FENCE(); } while (0)
#define SB() __builtin_amdgcn_sched_barrier(0)
#define ALIGN_AT(n) asm volatile(".p2align 8\n\t.rept %c0\n\ts_nop 0\n\t.endr" :: "n"(n))
#define AL_G0 0
#define AL_G1 0
#define AL_G2 0
#define AL_G3 0
#define AL_G4 0
#define AL_L 32
#define AL_P 0
#define AL_X 0
#define AL_C 0
#define AL_U 0
#define RSRC(p, n) __builtin_amdgcn_make_buffer_rsrc((void*)(p), 0, (n), 0x00020000)
#define DMA16(r, dst, voff, soff) __builtin_amdgcn_raw_ptr_buffer_load_lds(r, dst, 16, voff, soff, 0, 0)

__device__ __forceinline__ float bf2f(u16 v) { return __uint_as_float(((unsigned)v) << 16); }
typedef __bf16 bf16x2_t __attribute__((ext_vector_type(2)));
__device__ __forceinline__ unsigned cvt_pk_bf16(float lo, float hi) { return __builtin_bit_cast(unsigned, __builtin_convertvector(((f32x2){ lo, hi }), bf16x2_t)); }
__device__ __forceinline__ float rbf(float f) { return __uint_as_float(cvt_pk_bf16(f, f) << 16); }
__device__ __forceinline__ float lo16(unsigned v) { return __uint_as_float(v << 16); }
__device__ __forceinline__ float hi16(unsigned v) { return __uint_as_float(v & 0xffff0000u); }
__device__ __forceinline__ s16x4 tr_read(LAS const char* p) {
    typedef short v4i16_t __attribute__((ext_vector_type(4)));
    return __builtin_bit_cast(s16x4, __builtin_amdgcn_ds_read_tr16_b64_v4i16((LAS v4i16_t*)p));
}
__device__ __forceinline__ bf16x8 cat8(s16x4 lo, s16x4 hi) { return __builtin_shufflevector(lo, hi, 0, 1, 2, 3, 4, 5, 6, 7); }
#define stamp(st, i) do {} while (0)
#define stamp_flush(st) do {} while (0)
#define STAMP_DECL
#define TS(k) do {} while (0)
template <int CTRL, int RM> __device__ __forceinline__ float dppf(float old, float v) {
    return __int_as_float(__builtin_amdgcn_update_dpp(__float_as_int(old), __float_as_int(v), CTRL, RM, 0xf, false));
}
__device__ __forceinline__ float wave_max_nonneg(float v) {
    v = fmaxf(v, dppf<0x111, 0xf>(0.f, v)); v = fmaxf(v, dppf<0x112, 0xf>(0.f, v));
    v = fmaxf(v, dppf<0x114, 0xf>(0.f, v)); v = fmaxf(v, dppf<0x118, 0xf>(0.f, v));
    v = fmaxf(v, dppf<0x142, 0xa>(0.f, v)); v = fmaxf(v, dppf<0x143, 0xc>(0.f, v));
    return __int_as_float(__builtin_amdgcn_readlane(__float_as_int(v), 63));
}

__device__ __forceinline__ u64 flag_load(char* ws, int idx) { return __hip_atomic_load((GAS u64*)(ws + WS_FLAG) + idx, __ATOMIC_RELAXED, __HIP_MEMORY_SCOPE_AGENT); }
__device__ __forceinline__ void flag_set(char* ws, int idx, u64 fv, int lane) { if (lane == 0) __hip_atomic_store((GAS u64*)(ws + WS_FLAG) + idx, fv, __ATOMIC_RELAXED, __HIP_MEMORY_SCOPE_AGENT); }
__device__ __forceinline__ void flag_wait(char* ws, int idx, u64 first, u64 fv) {
    if (__all(first == fv)) return;
    const u64 t0 = __builtin_amdgcn_s_memrealtime();
    for (;;) {
        const u64 v = flag_load(ws, idx);
        if (__all(v == fv)) return;
        if (__builtin_amdgcn_s_memrealtime() - t0 > 400000ull) return;
        __builtin_amdgcn_s_sleep(1);
    }
}
__device__ __forceinline__ void lds_arrive(LAS char* lds, int word, int lane) {
    WAIT_L(0);
    if (lane == 0) __hip_atomic_fetch_add((LAS unsigned*)(lds + S_SYNC) + word, 1u, __ATOMIC_RELAXED, __HIP_MEMORY_SCOPE_WORKGROUP);
}
__device__ __forceinline__ void lds_wait(LAS char* lds, int word, unsigned n) {
    if ((unsigned)__builtin_amdgcn_readfirstlane(*(volatile LAS unsigned*)(lds + S_SYNC + 4 * word)) >= n) { FENCE(); return; }
    const u64 t0 = __builtin_amdgcn_s_memrealtime();
    while ((unsigned)__builtin_amdgcn_readfirstlane(*(volatile LAS unsigned*)(lds + S_SYNC + 4 * word)) < n) {
        if (__builtin_amdgcn_s_memrealtime() - t0 > 400000ull) break;
        __builtin_amdgcn_s_sleep(1);
    }
    FENCE();
}

typedef f32x2 cf;
__device__ __forceinline__ cf cmul(cf a, cf b) {
    cf t, r;
    asm("v_pk_mul_f32 %0, %1, %2 op_sel:[1,1] op_sel_hi:[1,0] neg_lo:[0,1]" : "=v"(t) : "v"(a), "v"(b));
    asm("v_pk_fma_f32 %0, %1, %2, %3 op_sel:[0,0,0] op_sel_hi:[0,1,1]" : "=v"(r) : "v"(a), "v"(b), "v"(t));
    return r;
}
template <int N> __device__ __forceinline__ void cmulN(cf (&r)[N], const cf (&a)[N], const cf (&b)[N]) {
    cf t[N];
#pragma unroll
    for (int q = 0; q < N; ++q) asm volatile("v_pk_mul_f32 %0, %1, %2 op_sel:[1,1] op_sel_hi:[1,0] neg_lo:[0,1]" : "=v"(t[q]) : "v"(a[q]), "v"(b[q]));
#pragma unroll
    for (int q = 0; q < N; ++q) asm volatile("v_pk_fma_f32 %0, %1, %2, %3 op_sel:[0,0,0] op_sel_hi:[0,1,1]" : "=v"(r[q]) : "v"(a[q]), "v"(b[q]), "v"(t[q]));
}
template <int OFF, int N> __device__ __forceinline__ void put_rows(unsigned base, const unsigned (&v)[N]) {
    static_assert(N == 8 || N == 9, "");
    unsigned keep;
    if constexpr (N == 8)
        asm volatile("s_mov_b32 %0, m0\n\ts_mov_b32 m0, %1\n\ts_nop 0\n\t"
                     "ds_write_addtid_b32 %2 offset:%c10\n\tds_write_addtid_b32 %3 offset:%c10+288\n\tds_write_addtid_b32 %4 offset:%c10+576\n\tds_write_addtid_b32 %5 offset:%c10+864\n\t"
                     "ds_write_addtid_b32 %6 offset:%c10+1152\n\tds_write_addtid_b32 %7 offset:%c10+1440\n\tds_write_addtid_b32 %8 offset:%c10+1728\n\tds_write_addtid_b32 %9 offset:%c10+2016\n\t"
                     "s_mov_b32 m0, %0"
                     : "=&s"(keep) : "s"(base), "v"(v[0]), "v"(v[1]), "v"(v[2]), "v"(v[3]), "v"(v[4]), "v"(v[5]), "v"(v[6]), "v"(v[7]), "n"(OFF) : "memory");
    else
        asm volatile("s_mov_b32 %0, m0\n\ts_mov_b32 m0, %1\n\ts_nop 0\n\t"
                     "ds_write_addtid_b32 %2 offset:%c11\n\tds_write_addtid_b32 %3 offset:%c11+288\n\tds_write_addtid_b32 %4 offset:%c11+576\n\tds_write_addtid_b32 %5 offset:%c11+864\n\t"
                     "ds_write_addtid_b32 %6 offset:%c11+1152\n\tds_write_addtid_b32 %7 offset:%c11+1440\n\tds_write_addtid_b32 %8 offset:%c11+1728\n\tds_write_addtid_b32 %9 offset:%c11+2016\n\t"
                     "ds_write_addtid_b32 %10 offset:%c11+2304\n\ts_mov_b32 m0, %0"
                     : "=&s"(keep) : "s"(base), "v"(v[0]), "v"(v[1]), "v"(v[2]), "v"(v[3]), "v"(v[4]), "v"(v[5]), "v"(v[6]), "v"(v[7]), "v"(v[8]), "n"(OFF) : "memory");
}
__device__ __forceinline__ cf zpow(float a2, float tr, float p) {
    const float e = __builtin_amdgcn_exp2f(a2 * p), r = __builtin_amdgcn_fractf(tr * p);
    return (cf){ e * __builtin_amdgcn_cosf(r), e * __builtin_amdgcn_sinf(r) };
}
__device__ __forceinline__ unsigned pk_f16(cf a) { f16x2 h = { (_Float16)a.x, (_Float16)a.y }; return __builtin_bit_cast(unsigned, h); }

template <int OFFB, class Hook>
__device__ __forceinline__ void kgen_task(float a2, float tr, cf C, unsigned mbase, LAS const char* sy, int lane, f32x4 (&tot)[3], Hook hook) {
    float mx = wave_max_nonneg(fmaxf(fabsf(C.x), fabsf(C.y)));
    int ex = (int)(__float_as_uint(mx) >> 23); ex = ex < 13 ? 13 : ex;
    const float scale = __uint_as_float((unsigned)(267 - ex) << 23), unscale = __uint_as_float((unsigned)(ex - 12) << 23);
    LAS const char* sx = sy;
    const int rd = (lane & 15) * PRS + (lane >> 4) * 16;
    const cf z1 = zpow(a2, tr, 1.f), z8 = zpow(a2, tr, 8.f), z128 = zpow(a2, tr, 128.f), z1024 = zpow(a2, tr, 1024.f);
    const cf C0 = C * scale, w8 = { z8.x, -z8.y };
    cf l1[5];
    { const cf a[5] = { z1, z128, w8, C0, C0 }, bq[5] = { z1, z128, w8, z1, z1024 }; cmulN<5>(l1, a, bq); }
    const cf z2 = l1[0], z256 = l1[1], y2 = l1[2], v1 = l1[3], t0 = l1[4];
    cf l2[9];
    { const cf a[9] = { z2, z256, y2, y2, C0, v1, C0, t0, t0 }, bq[9] = { z2, z256, w8, y2, z2, z2, z256, z128, z1024 }; cmulN<9>(l2, a, bq); }
    const cf z4 = l2[0], z512 = l2[1], y3 = l2[2], y4 = l2[3], v2 = l2[4], v3 = l2[5], s0 = l2[6], t1 = l2[7], t8 = l2[8];
    cf l3[13];
    { const cf a[13] = { w8, y2, y3, y4, C0, v1, v2, v3, s0, s0, C0, t0, t1 }, bq[13] = { y4, y4, y4, y4, z4, z4, z4, z4, z2, z4, z512, z256, z256 }; cmulN<13>(l3, a, bq); }
    const cf y8 = l3[3], s1 = l3[8], s2 = l3[9], p0 = l3[10], t2 = l3[11], t3 = l3[12];
    SB(); hook(); SB();
    cf l4[7];
    { const cf a[7] = { w8, y2, y3, y4, l3[0], l3[1], l3[2] }, bq[7] = { y8, y8, y8, y8, y8, y8, y8 }; cmulN<7>(l4, a, bq); }
    {
        const unsigned u0[8] = { pk_f16((cf){ 1.f, 0.f }), pk_f16(w8), pk_f16(y2), pk_f16(y3), pk_f16(y4), pk_f16(l3[0]), pk_f16(l3[1]), pk_f16(l3[2]) };
        const unsigned u1[8] = { pk_f16(y8), pk_f16(l4[0]), pk_f16(l4[1]), pk_f16(l4[2]), pk_f16(l4[3]), pk_f16(l4[4]), pk_f16(l4[5]), pk_f16(l4[6]) };
        SB(); put_rows<OFFB, 8>(mbase, u0); put_rows<OFFB + 8 * PRS, 8>(mbase, u1);
    }
    f16x8 yb[4], a0[4], a1[4], a2f[4];
#pragma unroll
    for (int ks = 0; ks < 4; ++ks) yb[ks] = *(LAS const f16x8*)(sy + rd + ks * 64);
    SB();
    cf l5[8];
    { const cf a[8] = { C0, v1, v2, v3, l3[4], l3[5], l3[6], l3[7] }, bq[8] = { z128, z128, z128, z128, z128, z128, z128, z128 }; cmulN<8>(l5, a, bq); }
    {
        unsigned u0[8] = { pk_f16(C0), pk_f16(v1), pk_f16(v2), pk_f16(v3), pk_f16(l3[4]), pk_f16(l3[5]), pk_f16(l3[6]), pk_f16(l3[7]) }, u1[8];
#pragma unroll
        for (int q = 0; q < 8; ++q) u1[q] = pk_f16(l5[q]);
        SB(); put_rows<OFFB, 8>(mbase, u0); put_rows<OFFB + 8 * PRS, 8>(mbase, u1);
    }
#define RDA(dst) _Pragma("unroll") for (int ks = 0; ks < 4; ++ks) dst[ks] = *(LAS const f16x8*)(sx + rd + ks * 64)
#define MMA(t, A, B) do { f32x4 c_ = { 0, 0, 0, 0 }; _Pragma("unroll") for (int ks = 0; ks < 4; ++ks) c_ = __builtin_amdgcn_mfma_f32_16x16x32_f16(A[ks], B[ks], c_, 0, 0, 0); tot[t] += c_ * unscale; } while (0)
    RDA(a0); SB();
    hook(); SB();
    cf l6[4];
    { const cf a[4] = { s1, p0, p0, t0 }, bq[4] = { z4, z4, z128, z512 }; cmulN<4>(l6, a, bq); }
    const cf s3 = l6[0], p1 = l6[1], p2 = l6[2], t4 = l6[3];
    cf l7[8];
    { const cf a[8] = { s0, s1, s2, s3, p1, t1, t2, t3 }, bq[8] = { z128, z128, z128, z128, z128, z512, z512, z512 }; cmulN<8>(l7, a, bq); }
    const cf p3 = l7[4];
    cf l8[4];
    { const cf a[4] = { p0, p1, p2, p3 }, bq[4] = { z256, z256, z256, z256 }; cmulN<4>(l8, a, bq); }
    {
        const unsigned u0[8] = { pk_f16(s0), pk_f16(s1), pk_f16(s2), pk_f16(s3), pk_f16(l7[0]), pk_f16(l7[1]), pk_f16(l7[2]), pk_f16(l7[3]) };
        const unsigned u1[8] = { pk_f16(p0), pk_f16(p1), pk_f16(p2), pk_f16(p3), pk_f16(l8[0]), pk_f16(l8[1]), pk_f16(l8[2]), pk_f16(l8[3]) };
        SB(); put_rows<OFFB, 8>(mbase, u0); put_rows<OFFB + 8 * PRS, 8>(mbase, u1);
    }
    RDA(a1); SB();
    {
        const unsigned u2[9] = { pk_f16(t0), pk_f16(t1), pk_f16(t2), pk_f16(t3), pk_f16(t4), pk_f16(l7[5]), pk_f16(l7[6]), pk_f16(l7[7]), pk_f16(t8) };
        SB(); put_rows<OFFB, 9>(mbase, u2);
    }
    RDA(a2f); SB();
    MMA(0, a0, yb); MMA(1, a1, yb); MMA(2, a2f, yb);
#undef RDA
#undef MMA
}
__device__ __forceinline__ float fexp(float v) { return __builtin_amdgcn_exp2f(v * 1.44269504089f); }
struct ModeP { float a2, tr; cf C; };
__device__ __forceinline__ ModeP mode_setup(u16 la, u16 ai, u16 cr, u16 ci, float dt) {
    const float ar = -rbf(fexp(bf2f(la)));
    return { (ar * dt) * 1.44269504089f, (bf2f(ai) * dt) * 0.15915494309f, (cf){ bf2f(cr) * dt, bf2f(ci) * dt } };
}

struct Frags { bf16x8 x[4], g[3], u[3]; };
__device__ __forceinline__ void expand_a(LAS char* kx, LAS char* kc, const f32x4 (&tot)[3], int lane) {
    const int bq = lane & 15, g = lane >> 4;
    *(LAS u32x2*)(kx + lane * 8) = (u32x2){0, 0};
    const unsigned w0 = cvt_pk_bf16(tot[1][0], tot[1][1]), w1 = cvt_pk_bf16(tot[1][2], tot[1][3]);
    const int q0 = g < 2 ? 64 * g + 4 * bq : 128 + 64 * (g - 2) + 2 * bq, q1 = g < 2 ? q0 + 2 : q0 + 32;
    *(LAS unsigned*)(kc + q0 * 2) = w0; *(LAS unsigned*)(kc + q1 * 2) = w1;
    const unsigned x0 = cvt_pk_bf16(tot[2][0], tot[2][1]), x1 = cvt_pk_bf16(tot[2][2], tot[2][3]);
    LAS u16* k2 = (LAS u16*)(kc + (256 + 64 * g + bq) * 2);
    k2[0] = (u16)x0; k2[16] = (u16)(x0 >> 16); k2[32] = (u16)x1; k2[48] = (u16)(x1 >> 16);
    const int p0 = (g < 2 ? 0 : 128) + 8 * bq + 4 * (g & 1);
    *(LAS u32x2*)(kx + (256 + p0) * 2) = (u32x2){ cvt_pk_bf16(tot[0][0], tot[0][1]), cvt_pk_bf16(tot[0][2], tot[0][3]) };
    FENCE();
}
__device__ __forceinline__ void expand_b(LAS char* kx, LAS const char* kc, int lane) {
    FENCE();
    const int q1 = 4 * (lane & 31), q2 = 128 + 2 * lane, q3 = 256 + lane;
    const u32x2 w1v = *(LAS const u32x2*)(kc + q1 * 2); const unsigned v4 = *(LAS const u16*)(kc + (q1 + 4) * 2);
    const unsigned w2v = *(LAS const unsigned*)(kc + q2 * 2), c2 = *(LAS const u16*)(kc + (q2 + 2) * 2);
    const unsigned A0 = *(LAS const u16*)(kc + q3 * 2), B0 = *(LAS const u16*)(kc + (q3 + 1) * 2);
    const unsigned A1 = *(LAS const u16*)(kc + (q3 + 64) * 2), B1 = *(LAS const u16*)(kc + (q3 + 65) * 2);
    if (lane < 32)
        *(LAS u32x4*)(kx + (256 + 8 * (32 + lane)) * 2) = (u32x4){ __builtin_amdgcn_perm(w1v.x, w1v.x, 0x01000100), __builtin_amdgcn_perm(w1v.y, w1v.x, 0x05040302),
                                                                 __builtin_amdgcn_perm(w1v.y, w1v.y, 0x01000100), __builtin_amdgcn_perm(v4, w1v.y, 0x05040302) };
    *(LAS u32x4*)(kx + (256 + 8 * (64 + lane)) * 2) = (u32x4){ __builtin_amdgcn_perm(w2v, w2v, 0x01000100), w2v, __builtin_amdgcn_perm(w2v, w2v, 0x03020302), __builtin_amdgcn_perm(c2, c2, 0x01000100) };
    { const unsigned AA = A0 | (A0 << 16), BB = B0 | (B0 << 16);
      *(LAS u32x4*)(kx + (256 + 8 * (128 + lane)) * 2) = (u32x4){ AA, AA, (lane & 1) ? BB : (A0 | (B0 << 16)), BB }; }
    { const unsigned AA = A1 | (A1 << 16), BB = B1 | (B1 << 16);
      *(LAS u32x4*)(kx + (256 + 8 * (192 + lane)) * 2) = (u32x4){ AA, AA, (lane & 1) ? BB : (A1 | (B1 << 16)), BB }; }
    FENCE();
}

#define END_MODE 0
__global__ __launch_bounds__(512, 2) void k_fused(const u16* __restrict__ x, const u16* __restrict__ logA, const u16* __restrict__ Aim, const u16* __restrict__ Cre,
                                                  const u16* __restrict__ Cim, const u16* __restrict__ logdt, const u16* __restrict__ Dskip,
                                                  const u16* __restrict__ wg, const u16* __restrict__ wu, const u16* __restrict__ wd, const u16* __restrict__ gw,
                                                  char* __restrict__ ws, u16* __restrict__ out) {
    extern __shared__ __attribute__((aligned(16))) char smem[];
    LAS char* lds = (LAS char*)smem;
    const int tid = threadIdx.x, wid = __builtin_amdgcn_readfirstlane(tid >> 6), lane = tid & 63;
    const int b = blockIdx.x, xcd = b & 7, j = b >> 3;
    const int tm = 8 * (xcd & 1) + (j & 7), tn = 4 * (xcd >> 1) + (j >> 3);
    const int row0 = tm * 128, f0 = tn * 96;
    const bool conv = b < 160;
    const int w = wid & 3, d = conv ? 4 * b + w : w;
    const unsigned mbase = (unsigned)(32768 + KSCR_W * wid);
    LAS char* kscr = lds + KSCR + KSCR_W * wid;
    u64* st = (u64*)(ws + WS_STAMP) + (size_t)(b * 8 + wid) * 32;
    STAMP_DECL;
    stamp(st, 0);
    TS(39);
    unsigned epoch_v;
#define EPOCH_LOAD() do { epoch_v = __hip_atomic_load((GAS unsigned*)(ws + WS_EPOCH), __ATOMIC_RELAXED, __HIP_MEMORY_SCOPE_AGENT); } while (0)
    unsigned T; u64 fv;
#define GET_TAG() do { T = (unsigned)__builtin_amdgcn_readfirstlane(epoch_v) + 1u; fv = ((u64)T << 32) | (unsigned)~T; } while (0)

    if (wid >= 4) {
        u16 pds;
#define PARAM_LOAD() do { SB(); EPOCH_LOAD(); pds = Dskip[d]; SB(); } while (0)
        f32x4 tot[3], rem[3]; u64 fp0 = 0; bool remote = false;
#define REM_LOAD() do { auto rkp = RSRC(ws + WS_KP, D * 3072); _Pragma("unroll") for (int t_ = 0; t_ < 3; ++t_) { const u32x4 v_ = __builtin_amdgcn_raw_buffer_load_b128(rkp, lane * 16, d * 3072 + t_ * 1024, SC1); rem[t_] = __builtin_bit_cast(f32x4, v_); } } while (0)
        u64 fu0 = 0; const int fui = FU + (d / 80) * 64 + lane;
        LAS char* kx = lds + S_KX + w * KX_W;
        if (w < 2) {
            ALIGN_AT(AL_L);
            __builtin_amdgcn_s_setprio(3);
            auto rx = RSRC(x, L * D * 2); auto rg = RSRC(wg, D * F * 2); auto ru = RSRC(wu, D * F * 2);
            const int vx = (row0 + 64 * w + (lane >> 3)) * 1280 + (((lane & 7) ^ (lane >> 3)) << 4);
            int vw[6];
#pragma unroll
            for (int i = 0; i < 6; ++i) { const int P = 64 * (6 * w + i) + lane, k = P / 12, cp = P % 12, c = cp ^ ((k & 8) >> 2); vw[i] = k * 3072 + f0 * 2 + c * 16; }
            const int rot = ((5 * (j & 7) + 10 * (j >> 3)) >> 2) % 10;
            auto issue = [&](int t_, int slot) {
                int kt = t_ + rot; kt = kt >= NKT ? kt - NKT : kt;
                LAS char* sb = lds + slot * STAGE;
#pragma unroll
                for (int i = 0; i < 8; ++i) DMA16(rx, sb + XOFF + (8 * w + i) * 1024, vx, kt * 128 + i * 10240);
#pragma unroll
                for (int i = 0; i < 6; ++i) {
                    DMA16(rg, sb + GOFF + (6 * w + i) * 1024, vw[i], kt * 64 * 3072);
                    DMA16(ru, sb + UOFF + (6 * w + i) * 1024, vw[i], kt * 64 * 3072);
                }
            };
            TS(51);
            issue(0, 0);
            TS(52);
            issue(1, 1);
            TS(40);
            WAIT_V(20);
            TS(43);
            BAR();
            TS(44);
            issue(2, 2);
            PARAM_LOAD();
            WAIT_V(22);
            BAR();
            issue(3, 0);
            GET_TAG();
            int slot = 1;
#pragma unroll 1
            for (int t = 2; t < NKT - 2; ++t) {
                WAIT_V(20);
                BAR();
                if (t == NKT - 3 && conv) fp0 = flag_load(ws, FP + d);
                issue(t + 2, slot);
                slot = slot == NSLOT - 1 ? 0 : slot + 1;
            }
            WAIT_V(20); BAR();
            if (conv) { fu0 = flag_load(ws, fui); remote = __all(fp0 == fv); if (remote) REM_LOAD(); }
            WAIT_V(0); BAR();
            __builtin_amdgcn_s_setprio(0);
        } else {
            ALIGN_AT(AL_P);
            const int par = w & 1, l0 = (b >> 3) * 64, d0 = (b & 7) * 80, tc = b & 7, sp = b >> 3;
            const int g = lane >> 4, q = (lane & 15) >> 2, p = lane & 3;
            {
                auto rsrc = par ? RSRC(wd, F * D * 2) : RSRC(x, L * D * 2);
#pragma unroll
                for (int i = 0; i < 11; ++i) {
                    const int P = 64 * i + lane, r = P / 10, c = P % 10, l = l0 - 1 + r;
                    const int vo = par ? (b < 192 && i < 10 ? ((64 * sp + r) * D + tc * 80) * 2 + c * 16 : 0x40000000) : ((l >= 0 && r < 65) ? (l * D + d0) * 2 + c * 16 : 0x40000000);
                    if (i < 10 || !par) DMA16(rsrc, lds + (par ? P_W : P_U) + i * 1024, vo, 0);
                }
            }
            PARAM_LOAD();
            TS(51);
            if (par && lane < 8) *(LAS unsigned*)(lds + S_SYNC + 4 * lane) = 0u;
            GET_TAG();
            WAIT_V(0);
            if (!par && l0 == 0 && lane < 10) *(LAS u32x4*)(lds + P_U + lane * 16) = (u32x4){0, 0, 0, 0};
            WAIT_L(0);
            TS(41);
            BAR();
            auto rur = RSRC(ws + WS_UR, 2 * D * URS * 2); auto rwf = RSRC(ws + WS_WDF, F * D * 2);
            LAS const char* pu0 = lds + P_U + (64 - par - 8 * (4 * (g & 1) + p) - q) * 160 + (g >> 1) * 8;
            const int so0 = ((par * D + d0 + 4 * (g >> 1) + (lane & 3)) * URS + 1984 - l0 + 8 * (4 * (g & 1) + ((lane & 15) >> 2))) * 2;
            LAS const char* pw0 = lds + P_W + (4 * g + q) * 160;
#pragma unroll
            for (int k = 0; k < 7; ++k) {
                if (k < 2) {
#pragma unroll
                    for (int cg = 5 * k; cg < 5 * k + 5; ++cg) {
                        LAS const char* pr = pu0 + cg * 16;
                        const bf16x8 v = cat8(tr_read(pr), tr_read(pr - 4 * 160));
                        __builtin_amdgcn_raw_buffer_store_b128(__builtin_bit_cast(u32x4, v), rur, so0 + cg * 8 * URS * 2, 0, SC1);
                    }
                    if (k == 1 && l0 == 0) {
#pragma unroll
                        for (int i = 0; i < 2; ++i) {
                            const int P = 64 * i + lane;
                            __builtin_amdgcn_raw_buffer_store_b128((u32x4){0, 0, 0, 0}, rur, P < 80 ? ((par * D + d0 + P) * URS + 2048) * 2 : 0x40000000, 0, SC1);
                        }
                    }
                }
                if (k == 3) { if (par && b < 192) WAIT_V(4); else WAIT_V(0); flag_set(ws, FU + ((b & 7) * 32 + (b >> 3)) * 2 + par, fv, lane); }
                if (k == 6 && par && b < 192) { WAIT_V(0); flag_set(ws, FW + tc * 32 + sp, fv, lane); }
                if (par && b < 192 && k >= 2 && k < 5) {
#pragma unroll
                    for (int i = 4 * (k - 2); i < 4 * (k - 2) + 4 && i < 10; ++i) {
                        const int s2 = i / 5, i5 = i % 5;
                        const int dc = i5 < 4 ? 32 * (i5 >> 1) + 8 * p + 4 * (i5 & 1) : 64 + 4 * p;
                        LAS const char* pr = pw0 + 32 * s2 * 160 + dc * 2;
                        const bf16x8 v = cat8(tr_read(pr), tr_read(pr + 16 * 160));
                        __builtin_amdgcn_raw_buffer_store_b128(__builtin_bit_cast(u32x4, v), rwf, lane * 16 + ((tc * 5 + i5) * 48 + 2 * sp + s2) * 1024, 0, SC1);
                    }
                }
                if (k == 6 && conv) fp0 = flag_load(ws, FP + d);
                BAR();
            }
            BAR();
            if (conv) { fu0 = flag_load(ws, fui); remote = __all(fp0 == fv); if (remote) REM_LOAD(); }
            BAR();
        }
        stamp(st, 1);
        TS(50);
        if (__builtin_expect(!conv, 0)) {
            const int task = (b - 160) * 4 + w;
            if (task < 256) {
                const int grow = 8 * task + (lane >> 3), part = lane & 7;
                float sum = 0.f;
#pragma unroll
                for (int i = 0; i < 10; ++i) {
                    const u32x4 xv = *(const u32x4*)(x + grow * D + 64 * i + 8 * part), gv = *(const u32x4*)(gw + 64 * i + 8 * part);
#pragma unroll
                    for (int e = 0; e < 4; ++e) { const unsigned xa = xv[e], ga = gv[e]; sum = __builtin_amdgcn_fdot2_f32_bf16(__builtin_bit_cast(bf16x2_t, xa), __builtin_bit_cast(bf16x2_t, ga), sum, false); }
                }
                sum += dppf<0xB1, 0xf>(0.f, sum); sum += dppf<0x4E, 0xf>(0.f, sum); sum += dppf<0x141, 0xf>(0.f, sum);
                const float z = rbf(sum), gt = rbf(1.f / (1.f + __expf(-z)));
                if (part == 0) __hip_atomic_store((GAS unsigned*)(ws + WS_G) + grow, __float_as_uint(gt), __ATOMIC_RELAXED, __HIP_MEMORY_SCOPE_AGENT);
                WAIT_V(0);
                flag_set(ws, FG + task, fv, lane);
            }
            if (b == 255 && w == 3) {
                const u64 t0 = __builtin_amdgcn_s_memrealtime();
                for (;;) {
                    bool ok = true;
#pragma unroll
                    for (int i = 0; i < 16; ++i) ok &= flag_load(ws, FH + i * 64 + lane) == fv;
                    if (__all(ok) || __builtin_amdgcn_s_memrealtime() - t0 > 400000ull) break;
                    __builtin_amdgcn_s_sleep(8);
                }
                if (lane == 0) __hip_atomic_store((GAS unsigned*)(ws + WS_EPOCH), T, __ATOMIC_RELAXED, __HIP_MEMORY_SCOPE_AGENT);
            }
            stamp_flush(st);
            return;
        }
        LAS char* wl = lds + S_UR + w * UR_W;
        auto ur_dma = [&]() {
            auto rur = RSRC(ws + WS_UR, 2 * D * URS * 2);
#pragma unroll
            for (int c = 0; c < 2; ++c)
#pragma unroll
                for (int q = 0; q < 4; ++q)
                    __builtin_amdgcn_raw_ptr_buffer_load_lds(rur, wl + c * URC + q * 1024, 16, lane * 16, (c * D + d) * URS * 2 + q * 1024, 0, SC1);
        };
        ALIGN_AT(AL_X);
        TS(53);
#pragma unroll
        for (int t = 0; t < 3; ++t) tot[t] = *(LAS const f32x4*)(kx + t * 1024 + lane * 16);
        if (!remote) { flag_wait(ws, FP + d, flag_load(ws, FP + d), fv); REM_LOAD(); }
#pragma unroll
        for (int t = 0; t < 3; ++t) tot[t] += rem[t];
        asm volatile("" : "+v"(tot[0]), "+v"(tot[1]), "+v"(tot[2]));
        TS(56);
        flag_wait(ws, fui, fu0, fv);
        TS(54);
        ur_dma();
        SB();
        TS(55);
        stamp(st, 8);
        expand_a(kx, lds + S_KC + 1024 * w, tot, lane);
        TS(57);
        expand_b(kx, lds + S_KC + 1024 * w, lane);
        TS(58);
        if (lane < 4) *(LAS u32x4*)(wl + (lane >> 1) * URC + 4096 + (lane & 1) * 16) = (u32x4){0, 0, 0, 0};
        const float dsk = bf2f(pds);
        stamp(st, 2);
        TS(59);
        WAIT_V(0); WAIT_L(0);
        TS(60);
        __builtin_amdgcn_s_setprio(CONV_PRIO);
        stamp(st, 3);
        ALIGN_AT(AL_C);
        f32x4 y[8];
#pragma unroll
        for (int c = 0; c < 8; ++c) y[c] = (f32x4){0, 0, 0, 0};
        {
            const int r = lane & 15, kq = lane >> 4;
            const int par = (r & 1) ^ 1;
            LAS const char* ab = wl + par * URC + (2047 - 32 * 63 - 16 * (kq >> 1) + 8 * (kq & 1) - r - par) * 2;
            LAS const char* bbp = kx + 512 + 32 * r - 32 * (kq >> 1) + 16 * (kq & 1) - 64 * 7;
            bf16x8 A[2][8], B[2][8];
            unsigned abm[8];
#pragma unroll
            for (int mu = 0; mu < 8; ++mu) { abm[mu] = (unsigned)(size_t)ab + (56 - 8 * mu) * 64; asm volatile("" : "+v"(abm[mu])); }
            auto ldab = [&](int set, int rho, int mu) {
                const u32x4a4 av = *(LAS const u32x4a4*)((LAS const char*)(size_t)abm[mu] + (7 - rho) * 64);
                A[set][mu] = __builtin_bit_cast(bf16x8, (u32x4)av);
                B[set][mu] = *(volatile LAS const bf16x8*)(bbp + (8 * mu - rho + 7) * 64);
            };
#pragma unroll
            for (int mu = 0; mu < 8; ++mu) ldab(0, 0, mu);
#pragma unroll
            for (int rho = 0; rho < 8; ++rho) {
                const int cur = rho & 1;
                int e = 0;
#pragma unroll
                for (int mu = 0; mu < 8; ++mu)
#pragma unroll
                    for (int c = mu; c < 8; ++c) {
                        y[c] = __builtin_amdgcn_mfma_f32_16x16x32_bf16(A[cur][mu], B[cur][c - mu], y[c], 0, 0, 0);
                        ++e;
                        if (rho < 7 && (e & 3) == 0 && e <= 32) { SB(); ldab(cur ^ 1, rho + 1, e / 4 - 1); SB(); }
                    }
            }
        }
        stamp(st, 4);
        ALIGN_AT(AL_U);
        {
            const int col = lane & 15, g = lane >> 4;
            auto rat = RSRC(ws + WS_AT, D * L * 2);
            u32x2 uu[8], o[8];
#pragma unroll
            for (int c = 0; c < 8; ++c) uu[c] = *(LAS const u32x2*)(wl + (2044 - (256 * c + 16 * col + 4 * g)) * 2);
#pragma unroll
            for (int c = 0; c < 8; ++c) {
                const unsigned y01 = cvt_pk_bf16(y[c][0], y[c][1]), y23 = cvt_pk_bf16(y[c][2], y[c][3]);
                const unsigned s01 = cvt_pk_bf16(hi16(uu[c].y) * dsk, lo16(uu[c].y) * dsk), s23 = cvt_pk_bf16(hi16(uu[c].x) * dsk, lo16(uu[c].x) * dsk);
                o[c] = (u32x2){ cvt_pk_bf16(lo16(y01) + lo16(s01), hi16(y01) + hi16(s01)), cvt_pk_bf16(lo16(y23) + lo16(s23), hi16(y23) + hi16(s23)) };
            }
#pragma unroll
            for (int c = 0; c < 8; c += 2) {
                const auto sx = __builtin_amdgcn_permlane16_swap(o[c].x, o[c + 1].x, false, false), sy = __builtin_amdgcn_permlane16_swap(o[c].y, o[c + 1].y, false, false);
                __builtin_amdgcn_raw_buffer_store_b128((u32x4){ sx[0], sy[0], sx[1], sy[1] }, rat, (d * L + 256 * (c + (g & 1)) + 16 * col + 8 * (g >> 1)) * 2, 0, SC1);
            }
            WAIT_V(0);
            flag_set(ws, FA + d, fv, lane);
        }
        stamp(st, 5);
        stamp_flush(st);
        return;
    }

    ALIGN_AT(AL_G0);
    EPOCH_LOAD();
    const int hv = (b - 160) * 4 + w;
    f32x4 kp0[3] = { {0, 0, 0, 0}, {0, 0, 0, 0}, {0, 0, 0, 0} }, kp1[3] = { {0, 0, 0, 0}, {0, 0, 0, 0}, {0, 0, 0, 0} };
    {
        const int ch0 = conv ? d : hv, ch1 = conv ? d : (hv < 256 ? hv + 384 : hv), g0 = conv ? 0 : 128, g1 = conv ? 64 : 128;
        const int o0 = ch0 * NM + g0 + lane, o1 = ch1 * NM + g1 + lane;
        const u16 pA0 = logA[o0], pI0 = Aim[o0], pR0 = Cre[o0], pC0 = Cim[o0], pA1 = logA[o1], pI1 = Aim[o1], pR1 = Cre[o1], pC1 = Cim[o1], pd0 = logdt[ch0], pd1 = logdt[ch1];
        const ModeP m0 = mode_setup(pA0, pI0, pR0, pC0, rbf(fexp(bf2f(pd0))));
        ModeP m1 = mode_setup(pA1, pI1, pR1, pC1, rbf(fexp(bf2f(pd1))));
        asm volatile("" : "+v"(m1.a2), "+v"(m1.tr), "+v"(m1.C));
        TS(40);
        kgen_task<49152>(m0.a2, m0.tr, m0.C, mbase, kscr, lane, kp0, [] {});
        TS(41);
        if (conv || hv < 256) kgen_task<49152>(m1.a2, m1.tr, m1.C, mbase, kscr, lane, kp1, [] {});
        TS(42);
        if (conv) {
            LAS char* kx = lds + S_KX + w * KX_W;
#pragma unroll
            for (int t = 0; t < 3; ++t) *(LAS f32x4*)(kx + t * 1024 + lane * 16) = kp0[t] + kp1[t];
        }
    }
    GET_TAG();
    WAIT_L(0);
    stamp(st, 8);
    TS(43);
    const int m = wid >> 1, n = wid & 1;
    const int tr2 = 8 * (xcd >> 1) + (j & 7), tc2 = 4 * (xcd & 1) + (j >> 3);
    f32x4 ag[4][3], au[4][3];
    u64 fw0 = 0;
    const int fwi = FW + tc2 * 32 + 6 * wid + (lane % 6);
    {
        const int kq = lane >> 4, q = (lane & 15) >> 2, p = lane & 3, bb = kq & 1;
        const int xa0 = XOFF + (64 * m + (lane & 15)) * 128 + ((kq ^ (lane & 7)) << 4);
        const int xa1 = xa0 ^ 64;
        const int wrow = GOFF + (8 * kq + q) * 192 + (p & 1) * 8 + (p >> 1) * 16 + n * 96;
        const int waE = wrow + bb * 32, waO = wrow - bb * 32;
        const int wa0 = (n & 1) ? waO : waE, wa1 = (n & 1) ? waE : waO;
#pragma unroll
        for (int i = 0; i < 4; ++i)
#pragma unroll
            for (int t = 0; t < 3; ++t) { ag[i][t] = (f32x4){0, 0, 0, 0}; au[i][t] = (f32x4){0, 0, 0, 0}; }
        Frags P, Q;
        auto rd = [&](Frags& Fr, LAS const char* sb, int ks, int r) {
            if (r < 2) {
                Fr.x[2 * r] = *(LAS const bf16x8*)(sb + (ks ? xa1 : xa0) + (2 * r) * 2048);
                Fr.x[2 * r + 1] = *(LAS const bf16x8*)(sb + (ks ? xa1 : xa0) + (2 * r + 1) * 2048);
            } else {
                const int tt = (r - 2) >> 1, up = (r - 2) & 1;
                LAS const char* pw = sb + ((tt & 1) ? wa1 : wa0) + tt * 32 + ks * 6144 + up * (UOFF - GOFF);
                const bf16x8 v = cat8(tr_read(pw), tr_read(pw + 768));
                if (up) Fr.u[tt] = v; else Fr.g[tt] = v;
            }
        };
        auto mm = [&](const Frags& Fr, int r) {
#pragma unroll
            for (int i = 0; i < 3; ++i) {
                const int e = 3 * r + i, tt = e >> 3, up = (e >> 2) & 1, mt = e & 3;
                if (up) au[mt][tt] = __builtin_amdgcn_mfma_f32_16x16x32_bf16(Fr.u[tt], Fr.x[mt], au[mt][tt], 0, 0, 0);
                else ag[mt][tt] = __builtin_amdgcn_mfma_f32_16x16x32_bf16(Fr.g[tt], Fr.x[mt], ag[mt][tt], 0, 0, 0);
            }
        };
        BAR();
        TS(45);
        stamp(st, 1);
        ALIGN_AT(AL_G1);
#pragma unroll
        for (int r = 0; r < 8; ++r) rd(P, lds, 0, r);
        SB();
#pragma unroll
        for (int r = 0; r < 8; ++r) { rd(Q, lds, 1, r); SB(); mm(P, r); SB(); }
        WAIT_L(0);
        if (!conv) {
            auto rkp = RSRC(ws + WS_KP, D * 3072);
#pragma unroll
            for (int t = 0; t < 3; ++t) {
                __builtin_amdgcn_raw_buffer_store_b128(__builtin_bit_cast(u32x4, kp0[t]), rkp, lane * 16 + hv * 3072 + t * 1024, 0, SC1);
                __builtin_amdgcn_raw_buffer_store_b128(__builtin_bit_cast(u32x4, kp1[t]), rkp, hv < 256 ? lane * 16 + (hv + 384) * 3072 + t * 1024 : 0x40000000, 0, SC1);
            }
        }
        int slot = 0;
#pragma unroll 1
        for (int t = 1; t < NKT; ++t) {
            TS(2 * t);
            BAR();
            TS(2 * t + 1);
            slot = slot == NSLOT - 1 ? 0 : slot + 1;
            LAS const char* sb = lds + slot * STAGE;
            if (t == NKT - 1) fw0 = flag_load(ws, fwi);
            if (t == 4 && !conv) {
                WAIT_V(0);
                flag_set(ws, FP + hv, fv, lane);
                if (hv < 256) flag_set(ws, FP + hv + 384, fv, lane);
            }
#pragma unroll
            for (int r = 0; r < 8; ++r) { mm(Q, r); SB(); rd(P, sb, 0, r); SB(); }
#pragma unroll
            for (int r = 0; r < 8; ++r) { rd(Q, sb, 1, r); SB(); mm(P, r); SB(); }
            WAIT_L(0);
        }
#pragma unroll
        for (int r = 0; r < 8; ++r) mm(Q, r);
    }
    lds_arrive(lds, Y_ARR0, lane);
    __builtin_amdgcn_s_setprio(1);
    stamp(st, 2);
    ALIGN_AT(AL_G2);
    constexpr int WDEP = 5;
    auto rw = RSRC(ws + WS_WDF, F * D * 2);
    const int vh = lane * 16, wb = (tc2 * 5 * 48 + 12 * wid) * 1024;
    u32x4 wf[WDEP][5];
    auto ldw = [&](int s) {
#pragma unroll
        for (int i = 0; i < 5; ++i) wf[s % WDEP][i] = __builtin_amdgcn_raw_buffer_load_b128(rw, vh, wb + (i * 48 + s) * 1024, SC1);
    };
    {
        auto rh = RSRC(ws + WS_H, L * F * 2);
        const int tp0 = (row0 + 64 * m) / 32, ft0 = (f0 + 48 * n) / 16;
#pragma unroll
        for (int ip = 0; ip < 2; ++ip)
#pragma unroll
            for (int tt = 0; tt < 3; ++tt) {
                u32x4 o;
#pragma unroll
                for (int e = 0; e < 2; ++e) {
                    const f32x4 g = ag[2 * ip + e][tt], u = au[2 * ip + e][tt];
#pragma unroll
                    for (int r = 0; r < 2; ++r) {
                        const f32x2 gg = { g[2 * r], g[2 * r + 1] }, uu = { u[2 * r], u[2 * r + 1] };
                        const f32x2 a = gg * -1.44269504089f;
                        f32x2 ex = { __builtin_amdgcn_exp2f(a.x), __builtin_amdgcn_exp2f(a.y) };
                        ex += 1.f;
                        const f32x2 rc = { __builtin_amdgcn_rcpf(ex.x), __builtin_amdgcn_rcpf(ex.y) };
                        const f32x2 h = (gg * uu) * rc;
                        o[2 * e + r] = cvt_pk_bf16(h.x, h.y);
                    }
                }
                __builtin_amdgcn_raw_buffer_store_b128(o, rh, (((tp0 + ip) * 96 + ft0 + tt) * 64 + lane) * 16, 0, SC1);
            }
        stamp(st, 3);
        WAIT_V(0);
        flag_set(ws, FH + ((tm * 2 + m) * 16 + tn) * 2 + n, fv, lane);
        __builtin_amdgcn_s_setprio(0);
        stamp(st, 11);
    }
    flag_wait(ws, fwi, fw0, fv);
    SB();
#pragma unroll
    for (int s = 0; s < WDEP; ++s) ldw(s);
    LAS char* wl2 = lds + (wid == 3 ? 115200 : wid * 25600);
    lds_wait(lds, Y_ARR0, 4);
#pragma unroll
    for (int s = 5; s < 10; ++s)
#pragma unroll
        for (int i = 0; i < 5; ++i) __builtin_amdgcn_raw_ptr_buffer_load_lds(rw, wl2 + ((s - 5) * 5 + i) * 1024, 16, vh, wb + (i * 48 + s) * 1024, 0, SC1);
    SB();
    f32x4 acc[5][4];
#pragma unroll
    for (int i = 0; i < 5; ++i)
#pragma unroll
        for (int mt = 0; mt < 4; ++mt) acc[i][mt] = (f32x4){0, 0, 0, 0};
    {
        const int fhi = FH + (tr2 * 16 + 4 * wid) * 2 + (lane & 7);
        flag_wait(ws, fhi, flag_load(ws, fhi), fv);
        stamp(st, 4);
        ALIGN_AT(AL_G3);
        auto rh = RSRC(ws + WS_H, L * F * 2);
        const int hb = (tr2 * 2 * 96 + 24 * wid) * 1024;
        u32x4 hf[3][4];
        auto ldh = [&](int s) {
            hf[s % 3][0] = __builtin_amdgcn_raw_buffer_load_b128(rh, vh, hb + (2 * s) * 1024, SC1);
            hf[s % 3][1] = __builtin_amdgcn_raw_buffer_load_b128(rh, vh, hb + (2 * s + 1) * 1024, SC1);
            hf[s % 3][2] = __builtin_amdgcn_raw_buffer_load_b128(rh, vh, hb + (96 + 2 * s) * 1024, SC1);
            hf[s % 3][3] = __builtin_amdgcn_raw_buffer_load_b128(rh, vh, hb + (96 + 2 * s + 1) * 1024, SC1);
        };
        ldh(0); ldh(1);
        SB();
#pragma unroll
        for (int s = 0; s < 12; ++s) {
            if (s + 2 < 12) ldh(s + 2);
            SB();
            bf16x8 hb4[4];
#pragma unroll
            for (int pr = 0; pr < 2; ++pr) {
                const u32x4 a = hf[s % 3][2 * pr], c = hf[s % 3][2 * pr + 1];
                hb4[2 * pr] = __builtin_bit_cast(bf16x8, (u32x4){ a.x, a.y, c.x, c.y });
                hb4[2 * pr + 1] = __builtin_bit_cast(bf16x8, (u32x4){ a.z, a.w, c.z, c.w });
            }
#pragma unroll
            for (int i = 0; i < 5; ++i)
#pragma unroll
                for (int mt = 0; mt < 4; ++mt)
                    acc[i][mt] = __builtin_amdgcn_mfma_f32_16x16x32_bf16(__builtin_bit_cast(bf16x8, wf[s % WDEP][i]), hb4[mt], acc[i][mt], 0, 0, 0);
            SB();
            if (s >= 4 && s < 9) {
#pragma unroll
                for (int i = 0; i < 5; ++i) wf[(s + 1) % WDEP][i] = *(LAS const u32x4*)(wl2 + ((s - 4) * 5 + i) * 1024 + lane * 16);
            }
            if (s == 5 || s == 6) ldw(s + WDEP);
            SB();
        }
    }
    lds_arrive(lds, Y_ARR2, lane);
    stamp(st, 5);
    ALIGN_AT(AL_G4);
    const int tok = lane & 15, g4 = lane >> 4, l = tr2 * 64 + 16 * wid + tok;
    float gt;
    {
        const int pc = wid + 4 * (lane >> 3);
        const int fai = lane < 24 && pc < 10 ? FA + 80 * tc2 + 8 * pc + (lane & 7) : FG + 8 * tr2 + 2 * wid + (lane & 1);
        flag_wait(ws, fai, flag_load(ws, fai), fv);
        gt = __uint_as_float(__hip_atomic_load((GAS unsigned*)(ws + WS_G) + l, __ATOMIC_RELAXED, __HIP_MEMORY_SCOPE_AGENT));
        lds_wait(lds, Y_ARR2, 4);
        auto rat = RSRC(ws + WS_AT, D * L * 2);
#pragma unroll
        for (int i = 0; i < 3; ++i) {
            const int pi = wid + 4 * i;
            if (pi < 10)
                __builtin_amdgcn_raw_ptr_buffer_load_lds(rat, lds + S_AST + pi * 1024, 16, ((tc2 * 80 + 8 * pi + (lane >> 3)) * L + tr2 * 64) * 2 + (((lane & 7) ^ (lane >> 3)) << 4), 0, 0, SC1);
        }
    }
    stamp(st, 6);
#pragma unroll
    for (int mt = 0; mt < 4; ++mt)
        if (mt != wid) {
            const int sl = (wid - mt + 3) & 3;
#pragma unroll
            for (int i = 0; i < 5; ++i) *(LAS f32x4*)(lds + ((mt * 3 + sl) * 5 + i) * 1024 + lane * 16) = acc[i][mt];
        }
    WAIT_V(0);
    stamp(st, 12);
    lds_arrive(lds, Y_ARR1, lane);
    lds_wait(lds, Y_ARR1, 4);
    stamp(st, 13);
    {
        const float og = rbf(1.f - gt);
        const int q = tok >> 2, p = tok & 3;
        unsigned o[10];
#pragma unroll
        for (int i = 0; i < 5; ++i) {
            f32x4 s = wid == 0 ? acc[i][0] : wid == 1 ? acc[i][1] : wid == 2 ? acc[i][2] : acc[i][3];
#pragma unroll
            for (int sl = 0; sl < 3; ++sl) s += *(LAS const f32x4*)(lds + ((wid * 3 + sl) * 5 + i) * 1024 + lane * 16);
            const int drow = (i < 4 ? 32 * (i >> 1) + 8 * g4 + 4 * (i & 1) : 64 + 4 * g4) + q;
            const u32x2 a2 = __builtin_bit_cast(u32x2, tr_read(lds + S_AST + drow * 128 + (((2 * wid + (p >> 1)) ^ (drow & 7)) << 4) + (p & 1) * 8));
            const unsigned b01 = cvt_pk_bf16(s[0], s[1]), b23 = cvt_pk_bf16(s[2], s[3]);
            const unsigned ga01 = cvt_pk_bf16(gt * lo16(a2.x), gt * hi16(a2.x)), ga23 = cvt_pk_bf16(gt * lo16(a2.y), gt * hi16(a2.y));
            const unsigned ob01 = cvt_pk_bf16(og * lo16(b01), og * hi16(b01)), ob23 = cvt_pk_bf16(og * lo16(b23), og * hi16(b23));
            o[2 * i] = cvt_pk_bf16(lo16(ga01) + lo16(ob01), hi16(ga01) + hi16(ob01));
            o[2 * i + 1] = cvt_pk_bf16(lo16(ga23) + lo16(ob23), hi16(ga23) + hi16(ob23));
        }
        auto ro = RSRC(out, L * D * 2);
        const int ob = (l * D + tc2 * 80) * 2;
        __builtin_amdgcn_raw_buffer_store_b128((u32x4){ o[0], o[1], o[2], o[3] }, ro, ob + 16 * g4, 0, 0);
        __builtin_amdgcn_raw_buffer_store_b128((u32x4){ o[4], o[5], o[6], o[7] }, ro, ob + 64 + 16 * g4, 0, 0);
        __builtin_amdgcn_raw_buffer_store_b64((u32x2){ o[8], o[9] }, ro, ob + 128 + 8 * g4, 0, 0);
    }
    stamp(st, 7);
    stamp_flush(st);
}

extern "C" void kernel_launch(void* const* d_in, const int* in_sizes, int n_in, void* d_out, int out_size, void* d_ws, size_t ws_size, hipStream_t stream) {
    (void)hipFuncSetAttribute((const void*)k_fused, hipFuncAttributeMaxDynamicSharedMemorySize, LDS_BYTES);
    k_fused<<<256, 512, LDS_BYTES, stream>>>((const u16*)d_in[0], (const u16*)d_in[1], (const u16*)d_in[2], (const u16*)d_in[3], (const u16*)d_in[4], (const u16*)d_in[5],
                                             (const u16*)d_in[6], (const u16*)d_in[8], (const u16*)d_in[9], (const u16*)d_in[10], (const u16*)d_in[11], (char*)d_ws, (u16*)d_out);
}
```
